```python
import torch

def _softmax(x, axis=-1):
    return torch.softmax(x, dim=axis)
def _gelu(x):
    return torch.nn.functional.gelu(x, approximate="tanh")
def _silu(x):
    return torch.nn.functional.silu(x)
def _sigmoid(x):
    return torch.sigmoid(x)
def _layernorm(x, axis=-1, eps=1e-5):
    m = x.mean(dim=axis, keepdim=True)
    v = x.var(dim=axis, keepdim=True, unbiased=False)
    return (x - m) / torch.sqrt(v + eps)
def _scatter_add(tgt, idx, src, axis=0):
    return tgt.clone().index_add_(axis, idx, src)
def _segment_sum(src, seg, n):
    out = torch.zeros((n,) + src.shape[1:], dtype=src.dtype)
    return out.index_add_(0, seg, src)
def _pad(x, pw):
    flat = tuple(v for pair in reversed(pw) for v in pair)
    return torch.nn.functional.pad(x, flat)

kernel_name = 'standalone__mha_softmax__near_ridge'

def setup_inputs():

    _w = (lambda a: (lambda t: t.to(torch.bfloat16) if t.is_floating_point() else t)(torch.as_tensor(a)))
    B, H, Sq, Sk, D = 8, 32, 304, 2048, 128
    return {
        "q": _w((lambda *s: torch.randn(*s, dtype=torch.bfloat16) * 0.8)(B, H, Sq, D)),
        "k": _w((lambda *s: torch.randn(*s, dtype=torch.bfloat16) * 0.8)(B, H, Sk, D)),
        "v": _w((lambda *s: torch.randn(*s, dtype=torch.bfloat16) * 0.05)(B, H, Sk, D)),
    }

def reference(q, k, v):
    scale = q.shape[-1] ** -0.5
    scores = torch.einsum("bhqd,bhkd->bhqk", q, k) * scale
    p = _softmax(scores, axis=-1)
    return torch.einsum("bhqk,bhkd->bhqd", p, v)
```

```cpp
#include <hip/hip_runtime.h>
#include <hip/hip_bf16.h>
#include <stdint.h>

typedef __attribute__((ext_vector_type(8))) short bf16x8;
typedef __attribute__((ext_vector_type(16))) float f32x16;

constexpr int B_ = 8, H_ = 32, SQ = 304, SK = 2048, D_ = 128;

__device__ __forceinline__ unsigned short f2bf(float f) {
  __hip_bfloat16 b = __float2bfloat16(f);
  return *reinterpret_cast<unsigned short*>(&b);
}

__global__ __launch_bounds__(256, 1) void simple_attn(const unsigned short* __restrict__ Q, const unsigned short* __restrict__ K,
                                                      const unsigned short* __restrict__ V, unsigned short* __restrict__ O) {
  __shared__ unsigned short vs[32 * 128];
  const int bh = blockIdx.x;
  const int tid = threadIdx.x, lane = tid & 63, wid = tid >> 6, l32 = lane & 31, hi = lane >> 5;
  const unsigned short* q = Q + (size_t)bh * SQ * D_;
  const unsigned short* k = K + (size_t)bh * SK * D_;
  const unsigned short* v = V + (size_t)bh * SK * D_;
  unsigned short* o = O + (size_t)bh * SQ * D_;
  const float C = 0.08838834764831845f * 1.4426950408889634f;
  for (int round = 0; round < 3; ++round) {
    const int qb = round * 4 + wid;
    int row = qb * 32 + l32; const bool rv = row < SQ; if (!rv) row = SQ - 1;
    bf16x8 qf[8];
#pragma unroll
    for (int ds = 0; ds < 8; ++ds) qf[ds] = *reinterpret_cast<const bf16x8*>(q + (size_t)row * D_ + ds * 16 + hi * 8);
    f32x16 acc[4];
#pragma unroll
    for (int db = 0; db < 4; ++db) for (int r = 0; r < 16; ++r) acc[db][r] = 0.f;
    float m = -INFINITY, l = 0.f;
    for (int k0 = 0; k0 < SK; k0 += 32) {
      __syncthreads();
      for (int i = tid; i < 32 * 16; i += 256) *reinterpret_cast<bf16x8*>(vs + i * 8) = *reinterpret_cast<const bf16x8*>(v + (size_t)k0 * D_ + i * 8);
      __syncthreads();
      f32x16 s; for (int r = 0; r < 16; ++r) s[r] = 0.f;
#pragma unroll
      for (int ds = 0; ds < 8; ++ds) {
        bf16x8 kf = *reinterpret_cast<const bf16x8*>(k + (size_t)(k0 + l32) * D_ + ds * 16 + hi * 8);
        s = __builtin_amdgcn_mfma_f32_32x32x16_bf16(kf, qf[ds], s, 0, 0, 0);
      }
      float mx = s[0];
      for (int r = 1; r < 16; ++r) mx = fmaxf(mx, s[r]);
      mx = fmaxf(mx, __shfl_xor(mx, 32));
      const float mn = fmaxf(m, mx);
      const float alpha = exp2f((m - mn) * C);
      m = mn;
      float ps = 0.f; float p[16];
      for (int r = 0; r < 16; ++r) { p[r] = exp2f((s[r] - mn) * C); ps += p[r]; }
      ps += __shfl_xor(ps, 32);
      l = l * alpha + ps;
#pragma unroll
      for (int db = 0; db < 4; ++db) for (int r = 0; r < 16; ++r) acc[db][r] *= alpha;
#pragma unroll
      for (int ks = 0; ks < 2; ++ks) {
        bf16x8 pf;
        for (int j = 0; j < 8; ++j) pf[j] = (short)f2bf(p[8 * ks + j]);
#pragma unroll
        for (int db = 0; db < 4; ++db) {
          bf16x8 vf;
          for (int j = 0; j < 8; ++j) vf[j] = (short)vs[(16 * ks + 8 * (j >> 2) + 4 * hi + (j & 3)) * 128 + db * 32 + l32];
          acc[db] = __builtin_amdgcn_mfma_f32_32x32x16_bf16(vf, pf, acc[db], 0, 0, 0);
        }
      }
    }
    if (qb < 10 && rv) {
      const float il = 1.f / l;
#pragma unroll
      for (int db = 0; db < 4; ++db)
        for (int r = 0; r < 16; ++r) o[(size_t)row * D_ + db * 32 + (r & 3) + 8 * (r >> 2) + 4 * hi] = f2bf(acc[db][r] * il);
    }
  }
}

extern "C" void kernel_launch(void* const* d_in, const int* in_sizes, int n_in, void* d_out, int out_size, void* d_ws, size_t ws_size, hipStream_t stream) {
  simple_attn<<<B_ * H_, 256, 0, stream>>>((const unsigned short*)d_in[0], (const unsigned short*)d_in[1], (const unsigned short*)d_in[2], (unsigned short*)d_out);
}
```

```cpp
#include <hip/hip_runtime.h>
#include <hip/hip_bf16.h>
#include <stdint.h>

typedef __attribute__((ext_vector_type(8))) short bf16x8;
typedef __attribute__((ext_vector_type(16))) float f32x16;

constexpr int SQ = 304, SK = 2048, D_ = 128;

__device__ __forceinline__ unsigned short f2bf(float f) {
  __hip_bfloat16 b = __float2bfloat16(f);
  return *reinterpret_cast<unsigned short*>(&b);
}

__device__ __forceinline__ void fallback_rows(const unsigned short* __restrict__ q, const unsigned short* __restrict__ k,
                                              const unsigned short* __restrict__ v, unsigned short* __restrict__ o,
                                              unsigned short* vs, int row0, int nrows) {
  unsigned lane;
  asm volatile("s_waitcnt vmcnt(0)\n\tv_mbcnt_lo_u32_b32 %0, -1, 0\n\tv_mbcnt_hi_u32_b32 %0, -1, %0" : "=v"(lane) :: "memory");
  const int l32 = lane & 31, hi = lane >> 5;
  const float C = 0.08838834764831845f * 1.4426950408889634f;
  for (int r0 = 0; r0 < nrows; r0 += 32) {
    const bool rv = (r0 + l32) < nrows;
    const int row = row0 + (rv ? (r0 + l32) : 0);
    bf16x8 qf[8];
#pragma unroll
    for (int ds = 0; ds < 8; ++ds) qf[ds] = *reinterpret_cast<const bf16x8*>(q + (size_t)row * D_ + ds * 16 + hi * 8);
    f32x16 acc[4];
#pragma unroll
    for (int db = 0; db < 4; ++db)
#pragma unroll
      for (int r = 0; r < 16; ++r) acc[db][r] = 0.f;
    float m = -INFINITY, l = 0.f;
    for (int k0 = 0; k0 < SK; k0 += 32) {
      asm volatile("s_waitcnt lgkmcnt(0)" ::: "memory");
#pragma unroll
      for (int i = 0; i < 8; ++i)
        *reinterpret_cast<bf16x8*>(vs + (i * 64 + lane) * 8) = *reinterpret_cast<const bf16x8*>(v + (size_t)k0 * D_ + (i * 64 + lane) * 8);
      asm volatile("s_waitcnt lgkmcnt(0)" ::: "memory");
      f32x16 s;
#pragma unroll
      for (int r = 0; r < 16; ++r) s[r] = 0.f;
#pragma unroll
      for (int ds = 0; ds < 8; ++ds) {
        bf16x8 kf = *reinterpret_cast<const bf16x8*>(k + (size_t)(k0 + l32) * D_ + ds * 16 + hi * 8);
        s = __builtin_amdgcn_mfma_f32_32x32x16_bf16(kf, qf[ds], s, 0, 0, 0);
      }
      float mx = s[0];
#pragma unroll
      for (int r = 1; r < 16; ++r) mx = fmaxf(mx, s[r]);
      mx = fmaxf(mx, __shfl_xor(mx, 32));
      const float mn = fmaxf(m, mx);
      const float alpha = exp2f((m - mn) * C);
      m = mn;
      float ps = 0.f; float p[16];
#pragma unroll
      for (int r = 0; r < 16; ++r) { p[r] = exp2f((s[r] - mn) * C); ps += p[r]; }
      ps += __shfl_xor(ps, 32);
      l = l * alpha + ps;
#pragma unroll
      for (int db = 0; db < 4; ++db)
#pragma unroll
        for (int r = 0; r < 16; ++r) acc[db][r] *= alpha;
#pragma unroll
      for (int ks = 0; ks < 2; ++ks) {
        bf16x8 pf;
#pragma unroll
        for (int j = 0; j < 8; ++j) pf[j] = (short)f2bf(p[8 * ks + j]);
#pragma unroll
        for (int db = 0; db < 4; ++db) {
          bf16x8 vf;
#pragma unroll
          for (int j = 0; j < 8; ++j) vf[j] = (short)vs[(16 * ks + 8 * (j >> 2) + 4 * hi + (j & 3)) * 128 + db * 32 + l32];
          acc[db] = __builtin_amdgcn_mfma_f32_32x32x16_bf16(vf, pf, acc[db], 0, 0, 0);
        }
      }
    }
    if (rv) {
      const float il = 1.f / l;
#pragma unroll
      for (int db = 0; db < 4; ++db)
#pragma unroll
        for (int r = 0; r < 16; ++r) o[(size_t)row * D_ + db * 32 + (r & 3) + 8 * (r >> 2) + 4 * hi] = f2bf(acc[db][r] * il);
    }
  }
}

__global__ __launch_bounds__(256, 1) void attn_fwd(const char* __restrict__ Q, const char* __restrict__ K, const char* __restrict__ V,
                                                   char* __restrict__ O ) {
  __shared__ __attribute__((aligned(1024))) char lds[163840];
  const unsigned bh = blockIdx.x;
  const unsigned wid = __builtin_amdgcn_readfirstlane(threadIdx.x >> 6);
  const unsigned ldsb = (unsigned)(size_t)(__attribute__((address_space(3))) char*)lds;
  const char* qp = Q + (size_t)bh * (SQ * D_ * 2);
  const char* kp = K + (size_t)bh * (SK * D_ * 2);
  const char* vp = V + (size_t)bh * (SK * D_ * 2);
  char* op = O + (size_t)bh * (SQ * D_ * 2);
  unsigned flag;

  asm volatile(
    "s_mov_b64 s[48:49], %[q]\n\t"
    "s_and_b32 s49, s49, 0xffff\n\t"
    "s_mov_b32 s50, 77824\n\t"
    "s_mov_b32 s51, 0x20000\n\t"
    "s_mov_b64 s[40:41], %[k]\n\t"
    "s_and_b32 s41, s41, 0xffff\n\t"
    "s_mov_b32 s42, 524288\n\t"
    "s_mov_b32 s43, 0x20000\n\t"
    "s_mov_b64 s[44:45], %[v]\n\t"
    "s_and_b32 s45, s45, 0xffff\n\t"
    "s_mov_b32 s46, 524288\n\t"
    "s_mov_b32 s47, 0x20000\n\t"
    "s_mov_b64 s[52:53], %[o]\n\t"
    "s_and_b32 s53, s53, 0xffff\n\t"
    "s_mov_b32 s54, 77824\n\t"
    "s_mov_b32 s55, 0x20000\n\t"
    "v_mbcnt_lo_u32_b32 v0, -1, 0\n\t"
    "v_mbcnt_hi_u32_b32 v0, -1, v0\n\t"
    "s_mul_i32 s58, %[wid], 20480\n\t"
    "v_and_b32 v9, 15, v0\n\t"
    "v_lshlrev_b32 v8, 8, v9\n\t"
    "v_lshrrev_b32 v10, 4, v0\n\t"
    "v_lshl_add_u32 v8, v10, 4, v8\n\t"
    "v_bfe_u32 v11, v0, 2, 2\n\t"
    "v_lshlrev_b32 v11, 8, v11\n\t"
    "v_lshl_add_u32 v11, v10, 6, v11\n\t"
    "v_and_b32 v12, 3, v0\n\t"
    "v_xor_b32 v13, 0, v12\n\t"
    "v_lshl_add_u32 v4, v13, 4, v11\n\t"
    "v_xor_b32 v13, 2, v12\n\t"
    "v_lshl_add_u32 v5, v13, 4, v11\n\t"
    "v_xor_b32 v13, 3, v12\n\t"
    "v_lshl_add_u32 v6, v13, 4, v11\n\t"
    "v_xor_b32 v13, 1, v12\n\t"
    "v_lshl_add_u32 v7, v13, 4, v11\n\t"
    "s_lshl_b32 s66, %[wid], 12\n\t"
    "s_add_u32 s67, %[lds], s66\n\t"
    "s_add_u32 m0, s67, 0\n\t"
    "s_add_u32 s59, s66, 0\n\t"
    "buffer_load_dwordx4 v4, s[40:43], s59 offen nt lds\n\t"
    "buffer_load_dwordx4 v5, s[40:43], s59 offen offset:1024 nt lds\n\t"
    "buffer_load_dwordx4 v6, s[40:43], s59 offen offset:2048 nt lds\n\t"
    "buffer_load_dwordx4 v7, s[40:43], s59 offen offset:3072 nt lds\n\t"
    "s_add_u32 m0, s67, s66\n\t"
    "s_add_u32 m0, m0, 131072\n\t"
    "s_nop 0\n\t"
    "buffer_load_dwordx4 v4, s[48:51], s58 offen nt lds\n\t"
    "buffer_load_dwordx4 v5, s[48:51], s58 offen offset:1024 nt lds\n\t"
    "buffer_load_dwordx4 v6, s[48:51], s58 offen offset:2048 nt lds\n\t"
    "buffer_load_dwordx4 v7, s[48:51], s58 offen offset:3072 nt lds\n\t"
    "s_add_u32 s58, s58, 4096\n\t"
    "s_add_u32 m0, s67, 65536\n\t"
    "s_add_u32 s59, s66, 0\n\t"
    "buffer_load_dwordx4 v4, s[44:47], s59 offen nt lds\n\t"
    "buffer_load_dwordx4 v5, s[44:47], s59 offen offset:1024 nt lds\n\t"
    "buffer_load_dwordx4 v4, s[44:47], s59 offen offset:2048 nt lds\n\t"
    "buffer_load_dwordx4 v5, s[44:47], s59 offen offset:3072 nt lds\n\t"
    "s_cmp_eq_u32 %[wid], 3\n\t"
    "s_cbranch_scc1 PATH4_%=\n\t"
    "v_lshrrev_b32 v11, 2, v9\n\t"
    "v_lshlrev_b32 v12, 10, v11\n\t"
    "v_and_b32 v13, 3, v9\n\t"
    "v_lshl_add_u32 v12, v13, 6, v12\n\t"
    "v_lshlrev_b32 v11, 1, v11\n\t"
    "v_mov_b32 v13, 0x78\n\t"
    "v_lshrrev_b32 v11, v11, v13\n\t"
    "v_and_b32 v11, 3, v11\n\t"
    "v_xor_b32 v11, v11, v10\n\t"
    "v_lshl_add_u32 v1, v11, 4, v12\n\t"
    "v_add_u32 v1, %[lds], v1\n\t"
    "v_lshlrev_b32 v12, 10, v10\n\t"
    "v_bfe_u32 v13, v0, 2, 2\n\t"
    "v_lshl_add_u32 v12, v13, 6, v12\n\t"
    "v_and_b32 v13, 3, v0\n\t"
    "v_lshl_add_u32 v12, v13, 3, v12\n\t"
    "v_and_b32 v13, 1, v10\n\t"
    "v_lshl_add_u32 v12, v13, 5, v12\n\t"
    "v_add_u32 v2, %[lds], v12\n\t"
    "v_add_u32 v2, 0x10000, v2\n\t"
    "v_xor_b32 v3, 32, v2\n\t"
    "v_mov_b32 v144, 0\n\t"
    "v_mov_b32 v145, 0\n\t"
    "v_mov_b32 v146, 0\n\t"
    "v_mov_b32 v147, 0\n\t"
    "v_mov_b32 v148, 0\n\t"
    "v_mov_b32 v149, 0\n\t"
    "v_mov_b32 v150, 0\n\t"
    "v_mov_b32 v151, 0\n\t"
    "v_mov_b32 v152, 0\n\t"
    "v_mov_b32 v153, 0\n\t"
    "v_mov_b32 v154, 0\n\t"
    "v_mov_b32 v155, 0\n\t"
    "v_mov_b32 v156, 0\n\t"
    "v_mov_b32 v157, 0\n\t"
    "v_mov_b32 v158, 0\n\t"
    "v_mov_b32 v159, 0\n\t"
    "v_mov_b32 v160, 0\n\t"
    "v_mov_b32 v161, 0\n\t"
    "v_mov_b32 v162, 0\n\t"
    "v_mov_b32 v163, 0\n\t"
    "v_mov_b32 v164, 0\n\t"
    "v_mov_b32 v165, 0\n\t"
    "v_mov_b32 v166, 0\n\t"
    "v_mov_b32 v167, 0\n\t"
    "v_mov_b32 v168, 0\n\t"
    "v_mov_b32 v169, 0\n\t"
    "v_mov_b32 v170, 0\n\t"
    "v_mov_b32 v171, 0\n\t"
    "v_mov_b32 v172, 0\n\t"
    "v_mov_b32 v173, 0\n\t"
    "v_mov_b32 v174, 0\n\t"
    "v_mov_b32 v175, 0\n\t"
    "v_lshrrev_b32 v11, 2, v9\n\t"
    "v_and_b32 v12, 1, v0\n\t"
    "v_lshlrev_b32 v12, 4, v12\n\t"
    "v_and_b32 v13, 2, v0\n\t"
    "v_cmp_eq_u32 vcc, v11, v10\n\t"
    "v_mov_b32 v14, 0x3e03\n\t"
    "v_lshlrev_b32 v14, v12, v14\n\t"
    "v_cndmask_b32 v14, 0, v14, vcc\n\t"
    "v_mov_b32 v16, v14\n\t"
    "v_mov_b32 v14, 0xb9d8\n\t"
    "v_lshlrev_b32 v14, v12, v14\n\t"
    "v_cndmask_b32 v14, 0, v14, vcc\n\t"
    "v_mov_b32 v17, v14\n\t"
    "v_cmp_eq_u32 vcc, 0, v13\n\t"
    "v_cndmask_b32 v14, 0, v16, vcc\n\t"
    "v_xor_b32 v15, v14, v16\n\t"
    "v_accvgpr_write_b32 a234, v14\n\t"
    "v_accvgpr_write_b32 a235, v15\n\t"
    "v_cndmask_b32 v14, 0, v17, vcc\n\t"
    "v_xor_b32 v15, v14, v17\n\t"
    "v_accvgpr_write_b32 a238, v14\n\t"
    "v_accvgpr_write_b32 a239, v15\n\t"
    "v_accvgpr_write_b32 a232, 0\n\t"
    "v_accvgpr_write_b32 a233, 0\n\t"
    "v_accvgpr_write_b32 a236, 0\n\t"
    "v_accvgpr_write_b32 a237, 0\n\t"
    "v_accvgpr_write_b32 a240, 0\n\t"
    "v_accvgpr_write_b32 a241, 0\n\t"
    "v_accvgpr_write_b32 a212, 0\n\t"
    "v_accvgpr_write_b32 a213, 0\n\t"
    "v_accvgpr_write_b32 a214, 0\n\t"
    "v_accvgpr_write_b32 a215, 0\n\t"
    "v_accvgpr_write_b32 a216, 0\n\t"
    "v_accvgpr_write_b32 a217, 0\n\t"
    "v_accvgpr_write_b32 a218, 0\n\t"
    "v_accvgpr_write_b32 a219, 0\n\t"
    "v_accvgpr_write_b32 a220, 0\n\t"
    "v_accvgpr_write_b32 a221, 0\n\t"
    "v_accvgpr_write_b32 a222, 0\n\t"
    "v_accvgpr_write_b32 a223, 0\n\t"
    "v_accvgpr_write_b32 a224, 0\n\t"
    "v_accvgpr_write_b32 a225, 0\n\t"
    "v_accvgpr_write_b32 a226, 0\n\t"
    "v_accvgpr_write_b32 a227, 0\n\t"
    "v_accvgpr_write_b32 a228, 0\n\t"
    "v_accvgpr_write_b32 a229, 0\n\t"
    "v_accvgpr_write_b32 a230, 0\n\t"
    "v_accvgpr_write_b32 a231, 0\n\t"
    "v_mov_b32 v12, 0x3f803f80\n\t"
    "v_accvgpr_write_b32 a208, v12\n\t"
    "v_accvgpr_write_b32 a209, v12\n\t"
    "v_accvgpr_write_b32 a210, v12\n\t"
    "v_accvgpr_write_b32 a211, v12\n\t"
    "s_mov_b32 s60, 0x3e0293ee\n\t"
    "s_mov_b32 s61, 0x3e0293ee\n\t"
    "v_lshlrev_b32 v14, 4, v0\n\t"
    "v_add_u32 v14, %[lds], v14\n\t"
    "v_add_u32 v14, 0x21000, v14\n\t"
    "v_add_u32 v14, s66, v14\n\t"
    "v_add_u32 v14, s66, v14\n\t"
    "v_and_b32 v11, 1, v0\n\t"
    "v_mov_b32 v12, 0x44444444\n\t"
    "v_mov_b32 v13, 0xeeeeeeee\n\t"
    "v_cmp_eq_u32 vcc, 1, v11\n\t"
    "v_cndmask_b32 v0, v12, v13, vcc\n\t"
    "v_add_u32 v9, s66, v1\n\t"
    "v_add_u32 v8, 0xc000, v9\n\t"
    "v_add_u32 v9, s66, v9\n\t"
    "v_add_u32 v9, 0x20000, v9\n\t"
    "s_add_u32 m0, s67, s66\n\t"
    "s_add_u32 m0, m0, 135168\n\t"
    "s_nop 0\n\t"
    "buffer_load_dwordx4 v4, s[48:51], s58 offen nt lds\n\t"
    "buffer_load_dwordx4 v5, s[48:51], s58 offen offset:1024 nt lds\n\t"
    "buffer_load_dwordx4 v6, s[48:51], s58 offen offset:2048 nt lds\n\t"
    "buffer_load_dwordx4 v7, s[48:51], s58 offen offset:3072 nt lds\n\t"
    "s_add_u32 s58, s58, 4096\n\t"
    "s_waitcnt vmcnt(8)\n\t"
    "ds_read_b128 a[128:131], v9 offset:0\n\t"
    "ds_read_b128 a[132:135], v9 offset:256\n\t"
    "ds_read_b128 a[136:139], v9 offset:512\n\t"
    "ds_read_b128 a[140:143], v9 offset:768\n\t"
    "s_barrier\n\t"
    "ds_read_b128 a[0:3], v1 offset:0\n\t"
    "ds_read_b128 a[4:7], v1 offset:256\n\t"
    "ds_read_b128 a[8:11], v1 offset:512\n\t"
    "ds_read_b128 a[12:15], v1 offset:768\n\t"
    "ds_read_b128 a[16:19], v1 offset:4096\n\t"
    "ds_read_b128 a[20:23], v1 offset:4352\n\t"
    "ds_read_b128 a[24:27], v1 offset:4608\n\t"
    "ds_read_b128 a[28:31], v1 offset:4864\n\t"
    "ds_read_b128 a[32:35], v1 offset:8192\n\t"
    "ds_read_b128 a[36:39], v1 offset:8448\n\t"
    "ds_read_b128 a[40:43], v1 offset:8704\n\t"
    "s_waitcnt lgkmcnt(7)\n\t"
    "s_add_u32 m0, s67, 49152\n\t"
    "s_nop 0\n\t"
    "ds_read_b128 a[48:51], v1 offset:12288\n\t"
    "buffer_load_dwordx4 v4, s[48:51], s58 offen nt lds\n\t"
    "ds_read_b128 a[56:59], v1 offset:12800\n\t"
    "v_mfma_f32_16x16x32_bf16 v[224:227], a[0:3], a[128:131], 0\n\t"
    "buffer_load_dwordx4 v5, s[48:51], s58 offen offset:1024 nt lds\n\t"
    "v_mfma_f32_16x16x32_bf16 v[224:227], a[4:7], a[132:135], v[224:227]\n\t"
    "buffer_load_dwordx4 v6, s[48:51], s58 offen offset:2048 nt lds\n\t"
    "v_mfma_f32_16x16x32_bf16 v[224:227], a[8:11], a[136:139], v[224:227]\n\t"
    "ds_read_b128 a[44:47], v1 offset:8960\n\t"
    "ds_read_b128 a[52:55], v1 offset:12544\n\t"
    "v_mfma_f32_16x16x32_bf16 v[224:227], a[12:15], a[140:143], v[224:227]\n\t"
    "buffer_load_dwordx4 v7, s[48:51], s58 offen offset:3072 nt lds\n\t"
    "v_mfma_f32_16x16x32_bf16 v[16:19], a[234:237], a[128:131], 0\n\t"
    "s_add_u32 s58, s58, 4096\n\t"
    "ds_read_b128 a[60:63], v1 offset:13056\n\t"
    "v_mfma_f32_16x16x32_bf16 v[16:19], a[238:241], a[128:131], v[16:19]\n\t"
    "s_add_u32 m0, s67, 114688\n\t"
    "s_nop 0\n\t"
    "v_mfma_f32_16x16x32_bf16 v[20:23], a[232:235], a[128:131], 0\n\t"
    "buffer_load_dwordx4 v4, s[48:51], s58 offen nt lds\n\t"
    "v_mfma_f32_16x16x32_bf16 v[20:23], a[236:239], a[128:131], v[20:23]\n\t"
    "v_max3_f32 v12, v224, v225, v226\n\t"
    "v_max_f32 v12, v12, v227\n\t"
    "v_mov_b32 v13, v12\n\t"
    "v_mfma_f32_16x16x32_bf16 v[24:27], a[234:237], a[132:135], 0\n\t"
    "buffer_load_dwordx4 v5, s[48:51], s58 offen offset:1024 nt lds\n\t"
    "buffer_load_dwordx4 v6, s[48:51], s58 offen offset:2048 nt lds\n\t"
    "v_mfma_f32_16x16x32_bf16 v[24:27], a[238:241], a[132:135], v[24:27]\n\t"
    "v_cvt_pk_bf16_f32 v16, v16, v17\n\t"
    "buffer_load_dwordx4 v7, s[48:51], s58 offen offset:3072 nt lds\n\t"
    "s_add_u32 s58, s58, 4096\n\t"
    "v_mfma_f32_16x16x32_bf16 v[28:31], a[232:235], a[132:135], 0\n\t"
    "v_permlane16_swap_b32 v12, v13\n\t"
    "v_max_f32 v12, v12, v13\n\t"
    "v_mov_b32 v13, v12\n\t"
    "v_mfma_f32_16x16x32_bf16 v[28:31], a[236:239], a[132:135], v[28:31]\n\t"
    "v_cvt_pk_bf16_f32 v18, v18, v19\n\t"
    "v_cvt_pk_bf16_f32 v20, v20, v21\n\t"
    "v_cvt_pk_bf16_f32 v22, v22, v23\n\t"
    "v_accvgpr_write_b32 a131, v22\n\t"
    "v_mfma_f32_16x16x32_bf16 v[32:35], a[234:237], a[136:139], 0\n\t"
    "v_permlane32_swap_b32 v12, v13\n\t"
    "v_max_f32 v12, v12, v13\n\t"
    "v_mul_f32 v227, 0xbe0293ee, v12\n\t"
    "v_mfma_f32_16x16x32_bf16 v[32:35], a[238:241], a[136:139], v[32:35]\n\t"
    "v_mul_f32 v225, 0xbe0293ee, v12\n\t"
    "v_cvt_pk_bf16_f32 v26, v26, v27\n\t"
    "v_accvgpr_write_b32 a133, v26\n\t"
    "v_mfma_f32_16x16x32_bf16 v[36:39], a[232:235], a[136:139], 0\n\t"
    "v_mul_f32 v226, 0xbe0293ee, v12\n\t"
    "v_accvgpr_write_b32 a128, v16\n\t"
    "v_cvt_pk_bf16_f32 v24, v24, v25\n\t"
    "v_mfma_f32_16x16x32_bf16 v[36:39], a[236:239], a[136:139], v[36:39]\n\t"
    "v_mul_f32 v224, 0xbe0293ee, v12\n\t"
    "v_accvgpr_write_b32 a129, v18\n\t"
    "v_cvt_pk_bf16_f32 v28, v28, v29\n\t"
    "v_mfma_f32_16x16x32_bf16 v[40:43], a[234:237], a[140:143], 0\n\t"
    "v_accvgpr_write_b32 a132, v24\n\t"
    "v_cvt_pk_bf16_f32 v30, v30, v31\n\t"
    "v_accvgpr_write_b32 a135, v30\n\t"
    "v_mfma_f32_16x16x32_bf16 v[40:43], a[238:241], a[140:143], v[40:43]\n\t"
    "v_cvt_pk_bf16_f32 v32, v32, v33\n\t"
    "v_cvt_pk_bf16_f32 v34, v34, v35\n\t"
    "v_accvgpr_write_b32 a136, v32\n\t"
    "v_mfma_f32_16x16x32_bf16 v[44:47], a[232:235], a[140:143], 0\n\t"
    "v_accvgpr_write_b32 a130, v20\n\t"
    "v_accvgpr_write_b32 a134, v28\n\t"
    "v_accvgpr_write_b32 a137, v34\n\t"
    "v_mfma_f32_16x16x32_bf16 v[44:47], a[236:239], a[140:143], v[44:47]\n\t"
    "v_cvt_pk_bf16_f32 v36, v36, v37\n\t"
    "v_cvt_pk_bf16_f32 v38, v38, v39\n\t"
    "v_cvt_pk_bf16_f32 v42, v42, v43\n\t"
    "s_waitcnt lgkmcnt(0)\n\t"
    "v_mfma_f32_16x16x32_bf16 v[176:179], a[0:3], a[128:131], v[224:227]\n\t"
    "v_accvgpr_write_b32 a138, v36\n\t"
    "v_accvgpr_write_b32 a139, v38\n\t"
    "v_cvt_pk_bf16_f32 v40, v40, v41\n\t"
    "v_cvt_pk_bf16_f32 v46, v46, v47\n\t"
    "v_mfma_f32_16x16x32_bf16 v[176:179], a[4:7], a[132:135], v[176:179]\n\t"
    "v_accvgpr_write_b32 a140, v40\n\t"
    "v_cvt_pk_bf16_f32 v44, v44, v45\n\t"
    "v_accvgpr_write_b32 a141, v42\n\t"
    "v_accvgpr_write_b32 a142, v44\n\t"
    "v_accvgpr_write_b32 a143, v46\n\t"
    "v_mfma_f32_16x16x32_bf16 v[176:179], a[8:11], a[136:139], v[176:179]\n\t"
    "s_nop 0\n\t"
    "v_mfma_f32_16x16x32_bf16 v[176:179], a[12:15], a[140:143], v[176:179]\n\t"
    "v_mfma_f32_16x16x32_bf16 v[180:183], a[16:19], a[128:131], v[224:227]\n\t"
    "v_mfma_f32_16x16x32_bf16 v[180:183], a[20:23], a[132:135], v[180:183]\n\t"
    "v_mfma_f32_16x16x32_bf16 v[180:183], a[24:27], a[136:139], v[180:183]\n\t"
    "v_mfma_f32_16x16x32_bf16 v[180:183], a[28:31], a[140:143], v[180:183]\n\t"
    "s_nop 3\n\t"
    "v_exp_f32 v176, v176\n\t"
    "v_mfma_f32_16x16x32_bf16 v[184:187], a[32:35], a[128:131], v[224:227]\n\t"
    "v_exp_f32 v179, v179\n\t"
    "v_mfma_f32_16x16x32_bf16 v[184:187], a[36:39], a[132:135], v[184:187]\n\t"
    "v_exp_f32 v178, v178\n\t"
    "v_mfma_f32_16x16x32_bf16 v[184:187], a[40:43], a[136:139], v[184:187]\n\t"
    "v_exp_f32 v177, v177\n\t"
    "v_cvt_pk_bf16_f32 v209, v178, v179\n\t"
    "v_mfma_f32_16x16x32_bf16 v[184:187], a[44:47], a[140:143], v[184:187]\n\t"
    "v_exp_f32 v182, v182\n\t"
    "v_mfma_f32_16x16x32_bf16 v[188:191], a[48:51], a[128:131], v[224:227]\n\t"
    "v_exp_f32 v183, v183\n\t"
    "s_nop 0\n\t"
    "v_cvt_pk_bf16_f32 v211, v182, v183\n\t"
    "v_mfma_f32_16x16x32_bf16 v[188:191], a[52:55], a[132:135], v[188:191]\n\t"
    "v_cvt_pk_bf16_f32 v208, v176, v177\n\t"
    "v_exp_f32 v180, v180\n\t"
    "v_mfma_f32_16x16x32_bf16 v[188:191], a[56:59], a[136:139], v[188:191]\n\t"
    "v_exp_f32 v181, v181\n\t"
    "s_nop 0\n\t"
    "v_cvt_pk_bf16_f32 v210, v180, v181\n\t"
    "v_mfma_f32_16x16x32_bf16 v[188:191], a[60:63], a[140:143], v[188:191]\n\t"
    "s_waitcnt vmcnt(8)\n\t"
    "s_barrier\n\t"
    "ds_read_b128 a[144:147], v9 offset:4096\n\t"
    "ds_read_b128 a[148:151], v9 offset:4352\n\t"
    "ds_read_b128 a[152:155], v9 offset:4608\n\t"
    "ds_read_b128 a[156:159], v9 offset:4864\n\t"
    "ds_read_b64_tr_b16 a[64:65], v2 offset:0\n\t"
    "ds_read_b64_tr_b16 a[66:67], v2 offset:4096\n\t"
    "ds_read_b64_tr_b16 a[68:69], v2 offset:8192\n\t"
    "ds_read_b64_tr_b16 a[70:71], v2 offset:12288\n\t"
    "ds_read_b64_tr_b16 a[72:73], v3 offset:0\n\t"
    "ds_read_b64_tr_b16 a[74:75], v3 offset:4096\n\t"
    "ds_read_b64_tr_b16 a[76:77], v3 offset:8192\n\t"
    "ds_read_b64_tr_b16 a[78:79], v3 offset:12288\n\t"
    "ds_read_b64_tr_b16 a[80:81], v2 offset:256\n\t"
    "ds_read_b64_tr_b16 a[82:83], v2 offset:4352\n\t"
    "ds_read_b64_tr_b16 a[84:85], v2 offset:8448\n\t"
    "s_waitcnt lgkmcnt(11)\n\t"
    "v_mfma_f32_16x16x32_bf16 v[228:231], a[0:3], a[144:147], 0\n\t"
    "v_exp_f32 v190, v190\n\t"
    "ds_read_b64_tr_b16 a[108:109], v3 offset:8704\n\t"
    "ds_read_b64_tr_b16 a[126:127], v3 offset:13056\n\t"
    "v_mfma_f32_16x16x32_bf16 v[228:231], a[4:7], a[148:151], v[228:231]\n\t"
    "s_add_u32 m0, s67, s66\n\t"
    "ds_read_b64_tr_b16 a[98:99], v2 offset:4608\n\t"
    "ds_read_b64_tr_b16 a[102:103], v2 offset:12800\n\t"
    "v_mfma_f32_16x16x32_bf16 v[228:231], a[8:11], a[152:155], v[228:231]\n\t"
    "s_add_u32 m0, m0, 131072\n\t"
    "ds_read_b64_tr_b16 a[106:107], v3 offset:4608\n\t"
    "ds_read_b64_tr_b16 a[120:121], v3 offset:768\n\t"
    "v_mfma_f32_16x16x32_bf16 v[228:231], a[12:15], a[156:159], v[228:231]\n\t"
    "s_nop 0\n\t"
    "buffer_load_dwordx4 v4, s[48:51], s58 offen nt lds\n\t"
    "buffer_load_dwordx4 v5, s[48:51], s58 offen offset:1024 nt lds\n\t"
    "v_mfma_f32_16x16x32_bf16 v[48:51], a[234:237], a[144:147], 0\n\t"
    "buffer_load_dwordx4 v6, s[48:51], s58 offen offset:2048 nt lds\n\t"
    "buffer_load_dwordx4 v7, s[48:51], s58 offen offset:3072 nt lds\n\t"
    "s_add_u32 s58, s58, 4096\n\t"
    "v_mfma_f32_16x16x32_bf16 v[48:51], a[238:241], a[144:147], v[48:51]\n\t"
    "v_exp_f32 v184, v184\n\t"
    "ds_read_b64_tr_b16 a[86:87], v2 offset:12544\n\t"
    "ds_read_b64_tr_b16 a[116:117], v2 offset:8960\n\t"
    "v_mfma_f32_16x16x32_bf16 v[52:55], a[232:235], a[144:147], 0\n\t"
    "v_exp_f32 v189, v189\n\t"
    "ds_read_b64_tr_b16 a[94:95], v3 offset:12544\n\t"
    "ds_read_b64_tr_b16 a[122:123], v3 offset:4864\n\t"
    "v_mfma_f32_16x16x32_bf16 v[52:55], a[236:239], a[144:147], v[52:55]\n\t"
    "v_max3_f32 v12, v228, v229, v230\n\t"
    "v_max_f32 v12, v12, v231\n\t"
    "v_mov_b32 v13, v12\n\t"
    "s_add_u32 m0, s67, 16384\n\t"
    "v_mfma_f32_16x16x32_bf16 v[56:59], a[234:237], a[148:151], 0\n\t"
    "v_exp_f32 v188, v188\n\t"
    "v_permlane16_swap_b32 v12, v13\n\t"
    "v_max_f32 v12, v12, v13\n\t"
    "v_mfma_f32_16x16x32_bf16 v[56:59], a[238:241], a[148:151], v[56:59]\n\t"
    "v_cvt_pk_bf16_f32 v214, v188, v189\n\t"
    "v_mov_b32 v13, v12\n\t"
    "s_nop 1\n\t"
    "v_permlane32_swap_b32 v12, v13\n\t"
    "v_mfma_f32_16x16x32_bf16 v[60:63], a[232:235], a[148:151], 0\n\t"
    "v_max_f32 v12, v12, v13\n\t"
    "v_mul_f32 v230, 0xbe0293ee, v12\n\t"
    "v_cvt_pk_bf16_f32 v50, v50, v51\n\t"
    "s_add_u32 s59, s66, 16384\n\t"
    "v_mfma_f32_16x16x32_bf16 v[60:63], a[236:239], a[148:151], v[60:63]\n\t"
    "v_mul_f32 v228, 0xbe0293ee, v12\n\t"
    "v_cvt_pk_bf16_f32 v48, v48, v49\n\t"
    "buffer_load_dwordx4 v4, s[40:43], s59 offen nt lds\n\t"
    "v_mfma_f32_16x16x32_bf16 v[64:67], a[234:237], a[152:155], 0\n\t"
    "v_cvt_pk_bf16_f32 v52, v52, v53\n\t"
    "v_cvt_pk_bf16_f32 v54, v54, v55\n\t"
    "buffer_load_dwordx4 v5, s[40:43], s59 offen offset:1024 nt lds\n\t"
    "v_mfma_f32_16x16x32_bf16 v[64:67], a[238:241], a[152:155], v[64:67]\n\t"
    "v_mul_f32 v229, 0xbe0293ee, v12\n\t"
    "v_accvgpr_write_b32 a146, v52\n\t"
    "buffer_load_dwordx4 v6, s[40:43], s59 offen offset:2048 nt lds\n\t"
    "v_mfma_f32_16x16x32_bf16 v[68:71], a[232:235], a[152:155], 0\n\t"
    "v_accvgpr_write_b32 a147, v54\n\t"
    "v_cvt_pk_bf16_f32 v56, v56, v57\n\t"
    "v_cvt_pk_bf16_f32 v58, v58, v59\n\t"
    "v_mfma_f32_16x16x32_bf16 v[68:71], a[236:239], a[152:155], v[68:71]\n\t"
    "v_accvgpr_write_b32 a145, v50\n\t"
    "v_accvgpr_write_b32 a148, v56\n\t"
    "v_cvt_pk_bf16_f32 v60, v60, v61\n\t"
    "v_cvt_pk_bf16_f32 v62, v62, v63\n\t"
    "v_mfma_f32_16x16x32_bf16 v[72:75], a[234:237], a[156:159], 0\n\t"
    "v_mul_f32 v231, 0xbe0293ee, v12\n\t"
    "v_accvgpr_write_b32 a144, v48\n\t"
    "buffer_load_dwordx4 v7, s[40:43], s59 offen offset:3072 nt lds\n\t"
    "v_mfma_f32_16x16x32_bf16 v[72:75], a[238:241], a[156:159], v[72:75]\n\t"
    "v_cvt_pk_bf16_f32 v64, v64, v65\n\t"
    "v_cvt_pk_bf16_f32 v66, v66, v67\n\t"
    "v_accvgpr_write_b32 a152, v64\n\t"
    "v_accvgpr_write_b32 a153, v66\n\t"
    "v_mfma_f32_16x16x32_bf16 v[76:79], a[232:235], a[156:159], 0\n\t"
    "v_accvgpr_write_b32 a149, v58\n\t"
    "v_accvgpr_write_b32 a150, v60\n\t"
    "v_accvgpr_write_b32 a151, v62\n\t"
    "v_mfma_f32_16x16x32_bf16 v[76:79], a[236:239], a[156:159], v[76:79]\n\t"
    "v_cvt_pk_bf16_f32 v68, v68, v69\n\t"
    "v_cvt_pk_bf16_f32 v70, v70, v71\n\t"
    "v_accvgpr_write_b32 a154, v68\n\t"
    "s_nop 4\n\t"
    "v_cvt_pk_bf16_f32 v78, v78, v79\n\t"
    "v_mfma_f32_16x16x32_bf16 v[192:195], a[0:3], a[144:147], v[228:231]\n\t"
    "v_accvgpr_write_b32 a155, v70\n\t"
    "v_cvt_pk_bf16_f32 v74, v74, v75\n\t"
    "v_accvgpr_write_b32 a157, v74\n\t"
    "v_accvgpr_write_b32 a159, v78\n\t"
    "v_mfma_f32_16x16x32_bf16 v[192:195], a[4:7], a[148:151], v[192:195]\n\t"
    "v_cvt_pk_bf16_f32 v72, v72, v73\n\t"
    "v_accvgpr_write_b32 a156, v72\n\t"
    "v_cvt_pk_bf16_f32 v76, v76, v77\n\t"
    "v_accvgpr_write_b32 a158, v76\n\t"
    "v_mfma_f32_16x16x32_bf16 v[192:195], a[8:11], a[152:155], v[192:195]\n\t"
    "s_waitcnt vmcnt(12)\n\t"
    "ds_read_b128 a[164:167], v8 offset:256\n\t"
    "s_add_u32 m0, s67, 81920\n\t"
    "v_mfma_f32_16x16x32_bf16 v[192:195], a[12:15], a[156:159], v[192:195]\n\t"
    "ds_read_b64_tr_b16 a[112:113], v2 offset:768\n\t"
    "ds_read_b128 a[168:171], v8 offset:512\n\t"
    "ds_read_b128 a[172:175], v8 offset:768\n\t"
    "v_mfma_f32_16x16x32_bf16 v[196:199], a[16:19], a[144:147], v[228:231]\n\t"
    "ds_read_b64_tr_b16 a[114:115], v2 offset:4864\n\t"
    "ds_read_b128 a[160:163], v8 offset:0\n\t"
    "v_mfma_f32_16x16x32_bf16 v[196:199], a[20:23], a[148:151], v[196:199]\n\t"
    "ds_read_b64_tr_b16 a[90:91], v3 offset:4352\n\t"
    "ds_read_b64_tr_b16 a[96:97], v2 offset:512\n\t"
    "v_mfma_f32_16x16x32_bf16 v[196:199], a[24:27], a[152:155], v[196:199]\n\t"
    "v_exp_f32 v187, v187\n\t"
    "s_add_u32 s59, s66, 16384\n\t"
    "v_mfma_f32_16x16x32_bf16 v[196:199], a[28:31], a[156:159], v[196:199]\n\t"
    "ds_read_b64_tr_b16 a[100:101], v2 offset:8704\n\t"
    "v_exp_f32 v192, v192\n\t"
    "v_mfma_f32_16x16x32_bf16 v[200:203], a[32:35], a[144:147], v[228:231]\n\t"
    "ds_read_b64_tr_b16 a[110:111], v3 offset:12800\n\t"
    "s_waitcnt lgkmcnt(4)\n\t"
    "v_mfma_f32_16x16x32_bf16 v[232:235], a[0:3], a[160:163], 0\n\t"
    "ds_read_b64_tr_b16 a[88:89], v3 offset:256\n\t"
    "v_exp_f32 v193, v193\n\t"
    "v_mfma_f32_16x16x32_bf16 v[232:235], a[4:7], a[164:167], v[232:235]\n\t"
    "buffer_load_dwordx4 v4, s[44:47], s59 offen nt lds\n\t"
    "v_cvt_pk_bf16_f32 v216, v192, v193\n\t"
    "v_mfma_f32_16x16x32_bf16 v[232:235], a[8:11], a[168:171], v[232:235]\n\t"
    "v_exp_f32 v186, v186\n\t"
    "v_add_u32 v8, 0x10000, v8\n\t"
    "v_mfma_f32_16x16x32_bf16 v[232:235], a[12:15], a[172:175], v[232:235]\n\t"
    "ds_read_b64_tr_b16 a[92:93], v3 offset:8448\n\t"
    "ds_read_b64_tr_b16 a[118:119], v2 offset:13056\n\t"
    "v_mfma_f32_16x16x32_bf16 v[80:83], a[234:237], a[160:163], 0\n\t"
    "v_exp_f32 v191, v191\n\t"
    "s_nop 0\n\t"
    "v_cvt_pk_bf16_f32 v215, v190, v191\n\t"
    "v_mfma_f32_16x16x32_bf16 v[80:83], a[238:241], a[160:163], v[80:83]\n\t"
    "v_cvt_pk_bf16_f32 v213, v186, v187\n\t"
    "ds_write_b128 v14, v[208:211] offset:0\n\t"
    "v_mfma_f32_16x16x32_bf16 v[84:87], a[232:235], a[160:163], 0\n\t"
    "v_exp_f32 v185, v185\n\t"
    "s_nop 0\n\t"
    "v_cvt_pk_bf16_f32 v212, v184, v185\n\t"
    "v_mfma_f32_16x16x32_bf16 v[84:87], a[236:239], a[160:163], v[84:87]\n\t"
    "ds_read_b64_tr_b16 a[104:105], v3 offset:512\n\t"
    "ds_read_b64_tr_b16 a[124:125], v3 offset:8960\n\t"
    "v_mfma_f32_16x16x32_bf16 v[88:91], a[234:237], a[164:167], 0\n\t"
    "v_max3_f32 v12, v232, v233, v234\n\t"
    "v_exp_f32 v199, v199\n\t"
    "v_mfma_f32_16x16x32_bf16 v[88:91], a[238:241], a[164:167], v[88:91]\n\t"
    "v_max_f32 v12, v12, v235\n\t"
    "v_mov_b32 v13, v12\n\t"
    "s_nop 0\n\t"
    "s_nop 0\n\t"
    "v_permlane16_swap_b32 v12, v13\n\t"
    "v_mfma_f32_16x16x32_bf16 v[92:95], a[232:235], a[164:167], 0\n\t"
    "v_max_f32 v12, v12, v13\n\t"
    "v_mov_b32 v13, v12\n\t"
    "v_cvt_pk_bf16_f32 v80, v80, v81\n\t"
    "v_mfma_f32_16x16x32_bf16 v[92:95], a[236:239], a[164:167], v[92:95]\n\t"
    "v_permlane32_swap_b32 v12, v13\n\t"
    "v_cvt_pk_bf16_f32 v86, v86, v87\n\t"
    "v_mfma_f32_16x16x32_bf16 v[200:203], a[36:39], a[148:151], v[200:203]\n\t"
    "v_accvgpr_write_b32 a163, v86\n\t"
    "buffer_load_dwordx4 v5, s[44:47], s59 offen offset:1024 nt lds\n\t"
    "v_mfma_f32_16x16x32_bf16 v[96:99], a[234:237], a[168:171], 0\n\t"
    "v_max_f32 v12, v12, v13\n\t"
    "v_mul_f32 v235, 0xbe0293ee, v12\n\t"
    "v_mfma_f32_16x16x32_bf16 v[96:99], a[238:241], a[168:171], v[96:99]\n\t"
    "v_cvt_pk_bf16_f32 v84, v84, v85\n\t"
    "v_cvt_pk_bf16_f32 v88, v88, v89\n\t"
    "v_cvt_pk_bf16_f32 v90, v90, v91\n\t"
    "v_mfma_f32_16x16x32_bf16 v[100:103], a[232:235], a[168:171], 0\n\t"
    "v_cvt_pk_bf16_f32 v82, v82, v83\n\t"
    "v_accvgpr_write_b32 a160, v80\n\t"
    "v_mfma_f32_16x16x32_bf16 v[100:103], a[236:239], a[168:171], v[100:103]\n\t"
    "v_cvt_pk_bf16_f32 v92, v92, v93\n\t"
    "buffer_load_dwordx4 v4, s[44:47], s59 offen offset:2048 nt lds\n\t"
    "v_mfma_f32_16x16x32_bf16 v[104:107], a[234:237], a[172:175], 0\n\t"
    "v_mul_f32 v233, 0xbe0293ee, v12\n\t"
    "v_cvt_pk_bf16_f32 v94, v94, v95\n\t"
    "v_mfma_f32_16x16x32_bf16 v[104:107], a[238:241], a[172:175], v[104:107]\n\t"
    "v_mul_f32 v232, 0xbe0293ee, v12\n\t"
    "v_cvt_pk_bf16_f32 v98, v98, v99\n\t"
    "v_mfma_f32_16x16x32_bf16 v[200:203], a[40:43], a[152:155], v[200:203]\n\t"
    "v_accvgpr_write_b32 a167, v94\n\t"
    "buffer_load_dwordx4 v5, s[44:47], s59 offen offset:3072 nt lds\n\t"
    "v_mfma_f32_16x16x32_bf16 v[108:111], a[232:235], a[172:175], 0\n\t"
    "v_cvt_pk_bf16_f32 v102, v102, v103\n\t"
    "v_exp_f32 v198, v198\n\t"
    "v_mfma_f32_16x16x32_bf16 v[108:111], a[236:239], a[172:175], v[108:111]\n\t"
    "v_accvgpr_write_b32 a166, v92\n\t"
    "v_cvt_pk_bf16_f32 v100, v100, v101\n\t"
    "v_mfma_f32_16x16x32_bf16 v[200:203], a[44:47], a[156:159], v[200:203]\n\t"
    "v_accvgpr_write_b32 a164, v88\n\t"
    "v_cvt_pk_bf16_f32 v104, v104, v105\n\t"
    "v_mfma_f32_16x16x32_bf16 v[204:207], a[48:51], a[144:147], v[228:231]\n\t"
    "v_accvgpr_write_b32 a165, v90\n\t"
    "s_nop 0\n\t"
    "v_cvt_pk_bf16_f32 v110, v110, v111\n\t"
    "v_mfma_f32_16x16x32_bf16 v[204:207], a[52:55], a[148:151], v[204:207]\n\t"
    "v_accvgpr_write_b32 a171, v102\n\t"
    "v_cvt_pk_bf16_f32 v108, v108, v109\n\t"
    "v_mfma_f32_16x16x32_bf16 v[204:207], a[56:59], a[152:155], v[204:207]\n\t"
    "v_accvgpr_write_b32 a170, v100\n\t"
    "v_accvgpr_write_b32 a174, v108\n\t"
    "s_waitcnt lgkmcnt(0)\n\t"
    "v_mfma_f32_16x16x32_bf16 v[204:207], a[60:63], a[156:159], v[204:207]\n\t"
    "v_exp_f32 v197, v197\n\t"
    "v_mfma_f32_16x16x32_bf16 v[16:19], v[208:211], a[64:67], 0\n\t"
    "v_mul_f32 v234, 0xbe0293ee, v12\n\t"
    "v_cvt_pk_bf16_f32 v96, v96, v97\n\t"
    "v_mfma_f32_16x16x32_bf16 v[16:19], v[212:215], a[68:71], v[16:19]\n\t"
    "v_cvt_pk_bf16_f32 v106, v106, v107\n\t"
    "v_accvgpr_write_b32 a175, v110\n\t"
    "v_mfma_f32_16x16x32_bf16 v[20:23], v[208:211], a[72:75], 0\n\t"
    "v_accvgpr_write_b32 a162, v84\n\t"
    "v_exp_f32 v194, v194\n\t"
    "v_mfma_f32_16x16x32_bf16 v[20:23], v[212:215], a[76:79], v[20:23]\n\t"
    "v_accvgpr_write_b32 a169, v98\n\t"
    "v_cvt_pk_bf16_f32 v219, v198, v199\n\t"
    "v_mfma_f32_16x16x32_bf16 v[24:27], v[208:211], a[80:83], 0\n\t"
    "ds_write_b128 v14, v[212:215] offset:1024\n\t"
    "v_mfma_f32_16x16x32_bf16 v[24:27], v[212:215], a[84:87], v[24:27]\n\t"
    "v_accvgpr_write_b32 a161, v82\n\t"
    "v_accvgpr_write_b32 a173, v106\n\t"
    "v_mfma_f32_16x16x32_bf16 v[28:31], v[208:211], a[88:91], 0\n\t"
    "v_exp_f32 v204, v204\n\t"
    "v_mfma_f32_16x16x32_bf16 v[28:31], v[212:215], a[92:95], v[28:31]\n\t"
    "v_accvgpr_write_b32 a168, v96\n\t"
    "v_accvgpr_write_b32 a172, v104\n\t"
    "v_smfmac_f32_16x16x64_bf16 a[212:215], a[208:211], v[208:215], v0\n\t"
    "s_waitcnt lgkmcnt(0)\n\t"
    "s_barrier\n\t"
    "v_mfma_f32_16x16x32_bf16 v[176:179], a[0:3], a[160:163], v[232:235]\n\t"
    "v_exp_f32 v203, v203\n\t"
    "v_mfma_f32_16x16x32_bf16 v[176:179], a[4:7], a[164:167], v[176:179]\n\t"
    "v_exp_f32 v206, v206\n\t"
    "v_mfma_f32_16x16x32_bf16 v[176:179], a[8:11], a[168:171], v[176:179]\n\t"
    "s_waitcnt vmcnt(12)\n\t"
    "ds_read_b128 a[176:179], v8 offset:0\n\t"
    "s_add_u32 m0, s67, 32768\n\t"
    "v_mfma_f32_16x16x32_bf16 v[176:179], a[12:15], a[172:175], v[176:179]\n\t"
    "ds_read_b128 a[188:191], v8 offset:768\n\t"
    "s_add_u32 s59, s66, 32768\n\t"
    "v_mfma_f32_16x16x32_bf16 v[180:183], a[16:19], a[160:163], v[232:235]\n\t"
    "ds_read_b128 a[180:183], v8 offset:256\n\t"
    "ds_read_b128 a[184:187], v8 offset:512\n\t"
    "v_mfma_f32_16x16x32_bf16 v[180:183], a[20:23], a[164:167], v[180:183]\n\t"
    "v_exp_f32 v196, v196\n\t"
    "v_mfma_f32_16x16x32_bf16 v[180:183], a[24:27], a[168:171], v[180:183]\n\t"
    "buffer_load_dwordx4 v4, s[40:43], s59 offen nt lds\n\t"
    "v_mfma_f32_16x16x32_bf16 v[180:183], a[28:31], a[172:175], v[180:183]\n\t"
    "v_exp_f32 v177, v177\n\t"
    "v_mfma_f32_16x16x32_bf16 v[184:187], a[32:35], a[160:163], v[232:235]\n\t"
    "s_waitcnt lgkmcnt(0)\n\t"
    "v_cvt_pk_bf16_f32 v218, v196, v197\n\t"
    "v_mfma_f32_16x16x32_bf16 v[236:239], a[0:3], a[176:179], 0\n\t"
    "v_exp_f32 v207, v207\n\t"
    "v_mfma_f32_16x16x32_bf16 v[236:239], a[4:7], a[180:183], v[236:239]\n\t"
    "v_exp_f32 v205, v205\n\t"
    "v_mfma_f32_16x16x32_bf16 v[236:239], a[8:11], a[184:187], v[236:239]\n\t"
    "v_exp_f32 v202, v202\n\t"
    "v_mfma_f32_16x16x32_bf16 v[236:239], a[12:15], a[188:191], v[236:239]\n\t"
    "v_cvt_pk_bf16_f32 v222, v204, v205\n\t"
    "buffer_load_dwordx4 v5, s[40:43], s59 offen offset:1024 nt lds\n\t"
    "v_mfma_f32_16x16x32_bf16 v[112:115], a[234:237], a[176:179], 0\n\t"
    "v_cvt_pk_bf16_f32 v223, v206, v207\n\t"
    "v_exp_f32 v176, v176\n\t"
    "v_mfma_f32_16x16x32_bf16 v[112:115], a[238:241], a[176:179], v[112:115]\n\t"
    "v_cvt_pk_bf16_f32 v221, v202, v203\n\t"
    "v_exp_f32 v182, v182\n\t"
    "v_mfma_f32_16x16x32_bf16 v[184:187], a[36:39], a[164:167], v[184:187]\n\t"
    "v_exp_f32 v195, v195\n\t"
    "v_mfma_f32_16x16x32_bf16 v[116:119], a[232:235], a[176:179], 0\n\t"
    "v_max3_f32 v12, v236, v237, v238\n\t"
    "v_exp_f32 v181, v181\n\t"
    "v_mfma_f32_16x16x32_bf16 v[116:119], a[236:239], a[176:179], v[116:119]\n\t"
    "v_max_f32 v12, v12, v239\n\t"
    "v_mov_b32 v13, v12\n\t"
    "s_nop 0\n\t"
    "s_nop 0\n\t"
    "v_permlane16_swap_b32 v12, v13\n\t"
    "v_mfma_f32_16x16x32_bf16 v[120:123], a[234:237], a[180:183], 0\n\t"
    "v_max_f32 v12, v12, v13\n\t"
    "v_cvt_pk_bf16_f32 v112, v112, v113\n\t"
    "v_mfma_f32_16x16x32_bf16 v[120:123], a[238:241], a[180:183], v[120:123]\n\t"
    "v_exp_f32 v201, v201\n\t"
    "v_mfma_f32_16x16x32_bf16 v[124:127], a[232:235], a[180:183], 0\n\t"
    "v_mov_b32 v13, v12\n\t"
    "v_cvt_pk_bf16_f32 v208, v176, v177\n\t"
    "v_mfma_f32_16x16x32_bf16 v[124:127], a[236:239], a[180:183], v[124:127]\n\t"
    "v_cvt_pk_bf16_f32 v217, v194, v195\n\t"
    "v_cvt_pk_bf16_f32 v116, v116, v117\n\t"
    "v_mfma_f32_16x16x32_bf16 v[184:187], a[40:43], a[168:171], v[184:187]\n\t"
    "v_permlane32_swap_b32 v12, v13\n\t"
    "v_max_f32 v12, v12, v13\n\t"
    "v_mfma_f32_16x16x32_bf16 v[184:187], a[44:47], a[172:175], v[184:187]\n\t"
    "v_cvt_pk_bf16_f32 v118, v118, v119\n\t"
    "v_cvt_pk_bf16_f32 v120, v120, v121\n\t"
    "v_mfma_f32_16x16x32_bf16 v[188:191], a[48:51], a[160:163], v[232:235]\n\t"
    "v_mul_f32 v239, 0xbe0293ee, v12\n\t"
    "v_accvgpr_write_b32 a180, v120\n\t"
    "v_mfma_f32_16x16x32_bf16 v[128:131], a[234:237], a[184:187], 0\n\t"
    "v_cvt_pk_bf16_f32 v122, v122, v123\n\t"
    "v_accvgpr_write_b32 a181, v122\n\t"
    "v_mfma_f32_16x16x32_bf16 v[128:131], a[238:241], a[184:187], v[128:131]\n\t"
    "v_exp_f32 v180, v180\n\t"
    "v_mfma_f32_16x16x32_bf16 v[188:191], a[52:55], a[164:167], v[188:191]\n\t"
    "buffer_load_dwordx4 v6, s[40:43], s59 offen offset:2048 nt lds\n\t"
    "v_mfma_f32_16x16x32_bf16 v[132:135], a[232:235], a[184:187], 0\n\t"
    "v_mul_f32 v236, 0xbe0293ee, v12\n\t"
    "v_cvt_pk_bf16_f32 v210, v180, v181\n\t"
    "v_mfma_f32_16x16x32_bf16 v[188:191], a[56:59], a[168:171], v[188:191]\n\t"
    "v_mul_f32 v238, 0xbe0293ee, v12\n\t"
    "v_cvt_pk_bf16_f32 v124, v124, v125\n\t"
    "v_mfma_f32_16x16x32_bf16 v[132:135], a[236:239], a[184:187], v[132:135]\n\t"
    "v_cvt_pk_bf16_f32 v128, v128, v129\n\t"
    "v_cvt_pk_bf16_f32 v130, v130, v131\n\t"
    "v_mfma_f32_16x16x32_bf16 v[136:139], a[234:237], a[188:191], 0\n\t"
    "v_exp_f32 v200, v200\n\t"
    "v_mfma_f32_16x16x32_bf16 v[188:191], a[60:63], a[172:175], v[188:191]\n\t"
    "v_mul_f32 v237, 0xbe0293ee, v12\n\t"
    "v_accvgpr_write_b32 a184, v128\n\t"
    "v_mfma_f32_16x16x32_bf16 v[136:139], a[238:241], a[188:191], v[136:139]\n\t"
    "buffer_load_dwordx4 v7, s[40:43], s59 offen offset:3072 nt lds\n\t"
    "v_mfma_f32_16x16x32_bf16 v[140:143], a[232:235], a[188:191], 0\n\t"
    "v_cvt_pk_bf16_f32 v220, v200, v201\n\t"
    "v_cvt_pk_bf16_f32 v126, v126, v127\n\t"
    "v_mfma_f32_16x16x32_bf16 v[48:51], v[216:219], a[64:67], 0\n\t"
    "v_accvgpr_write_b32 a178, v116\n\t"
    "v_cvt_pk_bf16_f32 v134, v134, v135\n\t"
    "v_mfma_f32_16x16x32_bf16 v[140:143], a[236:239], a[188:191], v[140:143]\n\t"
    "v_exp_f32 v179, v179\n\t"
    "v_mfma_f32_16x16x32_bf16 v[48:51], v[220:223], a[68:71], v[48:51]\n\t"
    "v_exp_f32 v190, v190\n\t"
    "v_mfma_f32_16x16x32_bf16 v[52:55], v[216:219], a[72:75], 0\n\t"
    "v_accvgpr_write_b32 a179, v118\n\t"
    "v_accvgpr_write_b32 a183, v126\n\t"
    "v_cvt_pk_bf16_f32 v132, v132, v133\n\t"
    "v_mfma_f32_16x16x32_bf16 v[52:55], v[220:223], a[76:79], v[52:55]\n\t"
    "v_cvt_pk_bf16_f32 v140, v140, v141\n\t"
    "v_exp_f32 v191, v191\n\t"
    "v_mfma_f32_16x16x32_bf16 v[56:59], v[216:219], a[80:83], 0\n\t"
    "v_exp_f32 v178, v178\n\t"
    "v_mfma_f32_16x16x32_bf16 v[56:59], v[220:223], a[84:87], v[56:59]\n\t"
    "v_accvgpr_write_b32 a186, v132\n\t"
    "v_cvt_pk_bf16_f32 v138, v138, v139\n\t"
    "v_mfma_f32_16x16x32_bf16 v[60:63], v[216:219], a[88:91], 0\n\t"
    "v_cvt_pk_bf16_f32 v142, v142, v143\n\t"
    "v_cvt_pk_bf16_f32 v209, v178, v179\n\t"
    "v_mfma_f32_16x16x32_bf16 v[60:63], v[220:223], a[92:95], v[60:63]\n\t"
    "v_exp_f32 v183, v183\n\t"
    "v_mfma_f32_16x16x32_bf16 v[64:67], v[216:219], a[96:99], 0\n\t"
    "v_accvgpr_write_b32 a189, v138\n\t"
    "v_accvgpr_write_b32 a191, v142\n\t"
    "v_mfma_f32_16x16x32_bf16 v[64:67], v[220:223], a[100:103], v[64:67]\n\t"
    "v_exp_f32 v188, v188\n\t"
    "v_mfma_f32_16x16x32_bf16 v[68:71], v[216:219], a[104:107], 0\n\t"
    "v_exp_f32 v189, v189\n\t"
    "v_mfma_f32_16x16x32_bf16 v[68:71], v[220:223], a[108:111], v[68:71]\n\t"
    "v_accvgpr_write_b32 a182, v124\n\t"
    "v_cvt_pk_bf16_f32 v136, v136, v137\n\t"
    "v_mfma_f32_16x16x32_bf16 v[72:75], v[216:219], a[112:115], 0\n\t"
    "v_cvt_pk_bf16_f32 v114, v114, v115\n\t"
    "v_accvgpr_write_b32 a190, v140\n\t"
    "v_mfma_f32_16x16x32_bf16 v[72:75], v[220:223], a[116:119], v[72:75]\n\t"
    "v_accvgpr_write_b32 a177, v114\n\t"
    "v_accvgpr_write_b32 a188, v136\n\t"
    "v_mfma_f32_16x16x32_bf16 v[76:79], v[216:219], a[120:123], 0\n\t"
    "v_accvgpr_write_b32 a176, v112\n\t"
    "v_accvgpr_write_b32 a185, v130\n\t"
    "v_mfma_f32_16x16x32_bf16 v[76:79], v[220:223], a[124:127], v[76:79]\n\t"
    "v_accvgpr_write_b32 a187, v134\n\t"
    "v_cvt_pk_bf16_f32 v211, v182, v183\n\t"
    "v_smfmac_f32_16x16x64_bf16 a[216:219], a[208:211], v[216:223], v0\n\t"
    "v_exp_f32 v187, v187\n\t"
    "v_mfma_f32_16x16x32_bf16 v[192:195], a[0:3], a[176:179], v[236:239]\n\t"
    "v_exp_f32 v185, v185\n\t"
    "v_mfma_f32_16x16x32_bf16 v[192:195], a[4:7], a[180:183], v[192:195]\n\t"
    "v_exp_f32 v186, v186\n\t"
    "v_mfma_f32_16x16x32_bf16 v[192:195], a[8:11], a[184:187], v[192:195]\n\t"
    "s_waitcnt vmcnt(12)\n\t"
    "s_add_u32 m0, s67, 98304\n\t"
    "v_mfma_f32_16x16x32_bf16 v[192:195], a[12:15], a[188:191], v[192:195]\n\t"
    "ds_read_b128 a[192:195], v9 offset:0\n\t"
    "ds_read_b128 a[196:199], v9 offset:256\n\t"
    "v_mfma_f32_16x16x32_bf16 v[196:199], a[16:19], a[176:179], v[236:239]\n\t"
    "ds_read_b128 a[200:203], v9 offset:512\n\t"
    "ds_read_b128 a[204:207], v9 offset:768\n\t"
    "v_mfma_f32_16x16x32_bf16 v[196:199], a[20:23], a[180:183], v[196:199]\n\t"
    "s_add_u32 s59, s66, 32768\n\t"
    "v_cvt_pk_bf16_f32 v214, v188, v189\n\t"
    "v_mfma_f32_16x16x32_bf16 v[196:199], a[24:27], a[184:187], v[196:199]\n\t"
    "v_exp_f32 v184, v184\n\t"
    "v_mfma_f32_16x16x32_bf16 v[196:199], a[28:31], a[188:191], v[196:199]\n\t"
    "v_exp_f32 v192, v192\n\t"
    "v_mfma_f32_16x16x32_bf16 v[200:203], a[32:35], a[176:179], v[236:239]\n\t"
    "s_waitcnt lgkmcnt(0)\n\t"
    "v_cvt_pk_bf16_f32 v213, v186, v187\n\t"
    "v_mfma_f32_16x16x32_bf16 v[240:243], a[0:3], a[192:195], 0\n\t"
    "buffer_load_dwordx4 v4, s[44:47], s59 offen nt lds\n\t"
    "v_mfma_f32_16x16x32_bf16 v[240:243], a[4:7], a[196:199], v[240:243]\n\t"
    "v_exp_f32 v193, v193\n\t"
    "v_mfma_f32_16x16x32_bf16 v[240:243], a[8:11], a[200:203], v[240:243]\n\t"
    "v_exp_f32 v197, v197\n\t"
    "v_mfma_f32_16x16x32_bf16 v[240:243], a[12:15], a[204:207], v[240:243]\n\t"
    "v_cvt_pk_bf16_f32 v212, v184, v185\n\t"
    "v_cvt_pk_bf16_f32 v216, v192, v193\n\t"
    "v_mfma_f32_16x16x32_bf16 v[112:115], a[234:237], a[192:195], 0\n\t"
    "buffer_load_dwordx4 v5, s[44:47], s59 offen offset:1024 nt lds\n\t"
    "v_mfma_f32_16x16x32_bf16 v[112:115], a[238:241], a[192:195], v[112:115]\n\t"
    "v_exp_f32 v194, v194\n\t"
    "v_mfma_f32_16x16x32_bf16 v[200:203], a[36:39], a[180:183], v[200:203]\n\t"
    "v_exp_f32 v195, v195\n\t"
    "v_mfma_f32_16x16x32_bf16 v[116:119], a[232:235], a[192:195], 0\n\t"
    "v_max3_f32 v12, v240, v241, v242\n\t"
    "v_max_f32 v12, v12, v243\n\t"
    "v_mfma_f32_16x16x32_bf16 v[116:119], a[236:239], a[192:195], v[116:119]\n\t"
    "v_mov_b32 v13, v12\n\t"
    "v_cvt_pk_bf16_f32 v215, v190, v191\n\t"
    "v_mfma_f32_16x16x32_bf16 v[120:123], a[234:237], a[196:199], 0\n\t"
    "v_cvt_pk_bf16_f32 v112, v112, v113\n\t"
    "v_exp_f32 v199, v199\n\t"
    "v_mfma_f32_16x16x32_bf16 v[120:123], a[238:241], a[196:199], v[120:123]\n\t"
    "v_permlane16_swap_b32 v12, v13\n\t"
    "v_max_f32 v12, v12, v13\n\t"
    "v_mfma_f32_16x16x32_bf16 v[124:127], a[232:235], a[196:199], 0\n\t"
    "v_mov_b32 v13, v12\n\t"
    "buffer_load_dwordx4 v4, s[44:47], s59 offen offset:2048 nt lds\n\t"
    "v_mfma_f32_16x16x32_bf16 v[124:127], a[236:239], a[196:199], v[124:127]\n\t"
    "v_permlane32_swap_b32 v12, v13\n\t"
    "v_exp_f32 v196, v196\n\t"
    "v_mfma_f32_16x16x32_bf16 v[200:203], a[40:43], a[184:187], v[200:203]\n\t"
    "v_exp_f32 v198, v198\n\t"
    "v_cvt_pk_bf16_f32 v218, v196, v197\n\t"
    "v_mfma_f32_16x16x32_bf16 v[200:203], a[44:47], a[188:191], v[200:203]\n\t"
    "v_max_f32 v12, v12, v13\n\t"
    "v_cvt_pk_bf16_f32 v114, v114, v115\n\t"
    "v_mfma_f32_16x16x32_bf16 v[204:207], a[48:51], a[176:179], v[236:239]\n\t"
    "v_cvt_pk_bf16_f32 v122, v122, v123\n\t"
    "v_accvgpr_write_b32 a197, v122\n\t"
    "v_mfma_f32_16x16x32_bf16 v[128:131], a[234:237], a[200:203], 0\n\t"
    "v_cvt_pk_bf16_f32 v116, v116, v117\n\t"
    "v_cvt_pk_bf16_f32 v126, v126, v127\n\t"
    "v_mfma_f32_16x16x32_bf16 v[128:131], a[238:241], a[200:203], v[128:131]\n\t"
    "v_accvgpr_write_b32 a194, v116\n\t"
    "v_cvt_pk_bf16_f32 v120, v120, v121\n\t"
    "v_cvt_pk_bf16_f32 v219, v198, v199\n\t"
    "v_mfma_f32_16x16x32_bf16 v[204:207], a[52:55], a[180:183], v[204:207]\n\t"
    "v_cvt_pk_bf16_f32 v118, v118, v119\n\t"
    "v_accvgpr_write_b32 a199, v126\n\t"
    "v_mfma_f32_16x16x32_bf16 v[132:135], a[232:235], a[200:203], 0\n\t"
    "v_accvgpr_write_b32 a193, v114\n\t"
    "v_cvt_pk_bf16_f32 v124, v124, v125\n\t"
    "v_mfma_f32_16x16x32_bf16 v[204:207], a[56:59], a[184:187], v[204:207]\n\t"
    "buffer_load_dwordx4 v5, s[44:47], s59 offen offset:3072 nt lds\n\t"
    "v_mfma_f32_16x16x32_bf16 v[132:135], a[236:239], a[200:203], v[132:135]\n\t"
    "v_accvgpr_write_b32 a198, v124\n\t"
    "v_cvt_pk_bf16_f32 v217, v194, v195\n\t"
    "v_mfma_f32_16x16x32_bf16 v[136:139], a[234:237], a[204:207], 0\n\t"
    "v_exp_f32 v203, v203\n\t"
    "v_mfma_f32_16x16x32_bf16 v[204:207], a[60:63], a[188:191], v[204:207]\n\t"
    "v_cvt_pk_bf16_f32 v128, v128, v129\n\t"
    "v_cvt_pk_bf16_f32 v130, v130, v131\n\t"
    "v_mfma_f32_16x16x32_bf16 v[136:139], a[238:241], a[204:207], v[136:139]\n\t"
    "v_mul_f32 v240, 0xbe0293ee, v12\n\t"
    "v_accvgpr_write_b32 a192, v112\n\t"
    "v_mfma_f32_16x16x32_bf16 v[140:143], a[232:235], a[204:207], 0\n\t"
    "v_mul_f32 v242, 0xbe0293ee, v12\n\t"
    "v_accvgpr_write_b32 a201, v130\n\t"
    "v_mfma_f32_16x16x32_bf16 v[80:83], v[208:211], a[64:67], 0\n\t"
    "v_mul_f32 v241, 0xbe0293ee, v12\n\t"
    "v_cvt_pk_bf16_f32 v132, v132, v133\n\t"
    "v_mfma_f32_16x16x32_bf16 v[140:143], a[236:239], a[204:207], v[140:143]\n\t"
    "v_exp_f32 v202, v202\n\t"
    "v_mfma_f32_16x16x32_bf16 v[80:83], v[212:215], a[68:71], v[80:83]\n\t"
    "v_cvt_pk_bf16_f32 v134, v134, v135\n\t"
    "v_cvt_pk_bf16_f32 v136, v136, v137\n\t"
    "v_mfma_f32_16x16x32_bf16 v[84:87], v[208:211], a[72:75], 0\n\t"
    "v_exp_f32 v207, v207\n\t"
    "v_mfma_f32_16x16x32_bf16 v[84:87], v[212:215], a[76:79], v[84:87]\n\t"
    "v_exp_f32 v205, v205\n\t"
    "v_mfma_f32_16x16x32_bf16 v[88:91], v[208:211], a[80:83], 0\n\t"
    "v_exp_f32 v206, v206\n\t"
    "v_mfma_f32_16x16x32_bf16 v[88:91], v[212:215], a[84:87], v[88:91]\n\t"
    "v_accvgpr_write_b32 a195, v118\n\t"
    "v_cvt_pk_bf16_f32 v138, v138, v139\n\t"
    "v_mfma_f32_16x16x32_bf16 v[92:95], v[208:211], a[88:91], 0\n\t"
    "v_accvgpr_write_b32 a196, v120\n\t"
    "v_cvt_pk_bf16_f32 v140, v140, v141\n\t"
    "v_mfma_f32_16x16x32_bf16 v[92:95], v[212:215], a[92:95], v[92:95]\n\t"
    "v_exp_f32 v204, v204\n\t"
    "v_mfma_f32_16x16x32_bf16 v[96:99], v[208:211], a[96:99], 0\n\t"
    "v_accvgpr_write_b32 a205, v138\n\t"
    "v_cvt_pk_bf16_f32 v142, v142, v143\n\t"
    "v_mfma_f32_16x16x32_bf16 v[96:99], v[212:215], a[100:103], v[96:99]\n\t"
    "v_accvgpr_write_b32 a204, v136\n\t"
    "v_accvgpr_write_b32 a206, v140\n\t"
    "v_mfma_f32_16x16x32_bf16 v[100:103], v[208:211], a[104:107], 0\n\t"
    "v_accvgpr_write_b32 a202, v132\n\t"
    "v_accvgpr_write_b32 a203, v134\n\t"
    "v_mfma_f32_16x16x32_bf16 v[100:103], v[212:215], a[108:111], v[100:103]\n\t"
    "v_accvgpr_write_b32 a200, v128\n\t"
    "v_cvt_pk_bf16_f32 v223, v206, v207\n\t"
    "v_mfma_f32_16x16x32_bf16 v[104:107], v[208:211], a[112:115], 0\n\t"
    "v_mul_f32 v243, 0xbe0293ee, v12\n\t"
    "v_accvgpr_write_b32 a207, v142\n\t"
    "v_mfma_f32_16x16x32_bf16 v[104:107], v[212:215], a[116:119], v[104:107]\n\t"
    "v_cvt_pk_bf16_f32 v221, v202, v203\n\t"
    "v_cvt_pk_bf16_f32 v222, v204, v205\n\t"
    "v_mfma_f32_16x16x32_bf16 v[108:111], v[208:211], a[120:123], 0\n\t"
    "v_exp_f32 v201, v201\n\t"
    "v_mfma_f32_16x16x32_bf16 v[108:111], v[212:215], a[124:127], v[108:111]\n\t"
    "v_exp_f32 v200, v200\n\t"
    "v_smfmac_f32_16x16x64_bf16 a[220:223], a[208:211], v[208:215], v0\n\t"
    "s_waitcnt vmcnt(8)\n\t"
    "s_barrier\n\t"
    "ds_read_b64_tr_b16 a[242:243], v3 offset:28672\n\t"
    "v_mfma_f32_16x16x32_bf16 v[176:179], a[0:3], a[192:195], v[240:243]\n\t"
    "s_add_u32 m0, s67, 49152\n\t"
    "ds_read_b64_tr_b16 a[244:245], v3 offset:16384\n\t"
    "v_mfma_f32_16x16x32_bf16 v[176:179], a[4:7], a[196:199], v[176:179]\n\t"
    "s_add_u32 s59, s66, 49152\n\t"
    "ds_read_b64_tr_b16 a[238:239], v2 offset:20736\n\t"
    "ds_read_b64_tr_b16 a[232:233], v2 offset:24832\n\t"
    "v_mfma_f32_16x16x32_bf16 v[176:179], a[8:11], a[200:203], v[176:179]\n\t"
    "ds_read_b64_tr_b16 a[254:255], v2 offset:20480\n\t"
    "buffer_load_dwordx4 v4, s[40:43], s59 offen nt lds\n\t"
    "v_mfma_f32_16x16x32_bf16 v[176:179], a[12:15], a[204:207], v[176:179]\n\t"
    "buffer_load_dwordx4 v5, s[40:43], s59 offen offset:1024 nt lds\n\t"
    "v_cvt_pk_bf16_f32 v220, v200, v201\n\t"
    "v_mfma_f32_16x16x32_bf16 v[180:183], a[16:19], a[192:195], v[240:243]\n\t"
    "ds_read_b128 a[8:11], v1 offset:16896\n\t"
    "ds_read_b64_tr_b16 v[252:253], v3 offset:16640\n\t"
    "v_mfma_f32_16x16x32_bf16 v[180:183], a[20:23], a[196:199], v[180:183]\n\t"
    "ds_read_b64_tr_b16 a[236:237], v2 offset:16640\n\t"
    "ds_read_b64_tr_b16 v[250:251], v3 offset:28928\n\t"
    "v_mfma_f32_16x16x32_bf16 v[180:183], a[24:27], a[200:203], v[180:183]\n\t"
    "buffer_load_dwordx4 v6, s[40:43], s59 offen offset:2048 nt lds\n\t"
    "ds_read_b128 a[0:3], v1 offset:16384\n\t"
    "v_mfma_f32_16x16x32_bf16 v[180:183], a[28:31], a[204:207], v[180:183]\n\t"
    "ds_read_b128 a[4:7], v1 offset:16640\n\t"
    "ds_read_b128 a[16:19], v1 offset:20480\n\t"
    "v_mfma_f32_16x16x32_bf16 v[184:187], a[32:35], a[192:195], v[240:243]\n\t"
    "ds_read_b64_tr_b16 a[240:241], v3 offset:24576\n\t"
    "ds_read_b128 a[24:27], v1 offset:20992\n\t"
    "v_mfma_f32_16x16x32_bf16 v[184:187], a[36:39], a[196:199], v[184:187]\n\t"
    "ds_read_b64_tr_b16 a[250:251], v2 offset:28672\n\t"
    "ds_read_b128 a[12:15], v1 offset:17152\n\t"
    "ds_read_b128 a[28:31], v1 offset:21248\n\t"
    "v_mfma_f32_16x16x32_bf16 v[184:187], a[40:43], a[200:203], v[184:187]\n\t"
    "buffer_load_dwordx4 v7, s[40:43], s59 offen offset:3072 nt lds\n\t"
    "s_add_u32 m0, s67, 114688\n\t"
    "v_mfma_f32_16x16x32_bf16 v[184:187], a[44:47], a[204:207], v[184:187]\n\t"
    "ds_read_b64_tr_b16 a[234:235], v2 offset:28928\n\t"
    "ds_read_b64_tr_b16 v[248:249], v3 offset:24832\n\t"
    "v_mfma_f32_16x16x32_bf16 v[188:191], a[48:51], a[192:195], v[240:243]\n\t"
    "ds_read_b64_tr_b16 a[248:249], v2 offset:24576\n\t"
    "ds_read_b128 a[20:23], v1 offset:20736\n\t"
    "v_mfma_f32_16x16x32_bf16 v[188:191], a[52:55], a[196:199], v[188:191]\n\t"
    "ds_read_b64_tr_b16 a[246:247], v3 offset:20480\n\t"
    "ds_read_b128 a[48:51], v1 offset:28672\n\t"
    "v_mfma_f32_16x16x32_bf16 v[188:191], a[56:59], a[200:203], v[188:191]\n\t"
    "s_add_u32 s59, s66, 49152\n\t"
    "ds_read_b128 a[36:39], v1 offset:24832\n\t"
    "v_mfma_f32_16x16x32_bf16 v[188:191], a[60:63], a[204:207], v[188:191]\n\t"
    "ds_read_b128 a[32:35], v1 offset:24576\n\t"
    "v_exp_f32 v182, v182\n\t"
    "v_mfma_f32_16x16x32_bf16 v[112:115], v[216:219], a[64:67], 0\n\t"
    "v_exp_f32 v183, v183\n\t"
    "v_mfma_f32_16x16x32_bf16 v[112:115], v[220:223], a[68:71], v[112:115]\n\t"
    "v_exp_f32 v180, v180\n\t"
    "v_cvt_pk_bf16_f32 v211, v182, v183\n\t"
    "v_mfma_f32_16x16x32_bf16 v[116:119], v[216:219], a[72:75], 0\n\t"
    "ds_read_b64_tr_b16 v[254:255], v3 offset:20736\n\t"
    "ds_read_b128 a[60:63], v1 offset:29440\n\t"
    "v_mfma_f32_16x16x32_bf16 v[116:119], v[220:223], a[76:79], v[116:119]\n\t"
    "buffer_load_dwordx4 v4, s[44:47], s59 offen nt lds\n\t"
    "v_mfma_f32_16x16x32_bf16 v[120:123], v[216:219], a[80:83], 0\n\t"
    "v_exp_f32 v181, v181\n\t"
    "v_mfma_f32_16x16x32_bf16 v[120:123], v[220:223], a[84:87], v[120:123]\n\t"
    "v_exp_f32 v184, v184\n\t"
    "v_mfma_f32_16x16x32_bf16 v[124:127], v[216:219], a[88:91], 0\n\t"
    "buffer_load_dwordx4 v5, s[44:47], s59 offen offset:1024 nt lds\n\t"
    "v_mfma_f32_16x16x32_bf16 v[124:127], v[220:223], a[92:95], v[124:127]\n\t"
    "buffer_load_dwordx4 v4, s[44:47], s59 offen offset:2048 nt lds\n\t"
    "v_mfma_f32_16x16x32_bf16 v[128:131], v[216:219], a[96:99], 0\n\t"
    "v_exp_f32 v190, v190\n\t"
    "v_mfma_f32_16x16x32_bf16 v[128:131], v[220:223], a[100:103], v[128:131]\n\t"
    "v_exp_f32 v185, v185\n\t"
    "v_mfma_f32_16x16x32_bf16 v[132:135], v[216:219], a[104:107], 0\n\t"
    "ds_read_b64_tr_b16 a[252:253], v2 offset:16384\n\t"
    "ds_read_b128 a[56:59], v1 offset:29184\n\t"
    "v_mfma_f32_16x16x32_bf16 v[132:135], v[220:223], a[108:111], v[132:135]\n\t"
    "v_exp_f32 v177, v177\n\t"
    "v_mfma_f32_16x16x32_bf16 v[136:139], v[216:219], a[112:115], 0\n\t"
    "ds_read_b128 a[40:43], v1 offset:25088\n\t"
    "ds_read_b128 a[52:55], v1 offset:28928\n\t"
    "v_mfma_f32_16x16x32_bf16 v[136:139], v[220:223], a[116:119], v[136:139]\n\t"
    "buffer_load_dwordx4 v5, s[44:47], s59 offen offset:3072 nt lds\n\t"
    "v_mfma_f32_16x16x32_bf16 v[140:143], v[216:219], a[120:123], 0\n\t"
    "v_exp_f32 v176, v176\n\t"
    "v_mfma_f32_16x16x32_bf16 v[140:143], v[220:223], a[124:127], v[140:143]\n\t"
    "ds_read_b128 a[44:47], v1 offset:25344\n\t"
    "s_waitcnt lgkmcnt(11)\n\t"
    "v_smfmac_f32_16x16x64_bf16 a[224:227], a[208:211], v[216:223], v0\n\t"
    "v_cvt_pk_bf16_f32 v210, v180, v181\n\t"
    "v_cvt_pk_bf16_f32 v212, v184, v185\n\t"
    "s_mov_b32 s62, 14\n\t"
    "LOOP5_%=:\n\t"
    "v_mfma_f32_16x16x32_bf16 v[192:195], a[0:3], a[128:131], v[224:227]\n\t"
    "v_exp_f32 v189, v189\n\t"
    "v_mfma_f32_16x16x32_bf16 v[192:195], a[4:7], a[132:135], v[192:195]\n\t"
    "v_exp_f32 v179, v179\n\t"
    "v_mfma_f32_16x16x32_bf16 v[192:195], a[8:11], a[136:139], v[192:195]\n\t"
    "v_exp_f32 v178, v178\n\t"
    "v_mfma_f32_16x16x32_bf16 v[192:195], a[12:15], a[140:143], v[192:195]\n\t"
    "v_exp_f32 v186, v186\n\t"
    "v_mfma_f32_16x16x32_bf16 v[196:199], a[16:19], a[128:131], v[224:227]\n\t"
    "v_exp_f32 v191, v191\n\t"
    "v_mfma_f32_16x16x32_bf16 v[196:199], a[20:23], a[132:135], v[196:199]\n\t"
    "v_cvt_pk_bf16_f32 v215, v190, v191\n\t"
    "v_mfma_f32_16x16x32_bf16 v[196:199], a[24:27], a[136:139], v[196:199]\n\t"
    "v_exp_f32 v187, v187\n\t"
    "v_mfma_f32_16x16x32_bf16 v[196:199], a[28:31], a[140:143], v[196:199]\n\t"
    "v_cvt_pk_bf16_f32 v208, v176, v177\n\t"
    "s_waitcnt lgkmcnt(0)\n\t"
    "v_mfma_f32_16x16x32_bf16 v[200:203], a[32:35], a[128:131], v[224:227]\n\t"
    "v_exp_f32 v188, v188\n\t"
    "v_mfma_f32_16x16x32_bf16 v[200:203], a[36:39], a[132:135], v[200:203]\n\t"
    "v_cvt_pk_bf16_f32 v214, v188, v189\n\t"
    "v_mfma_f32_16x16x32_bf16 v[200:203], a[40:43], a[136:139], v[200:203]\n\t"
    "v_exp_f32 v193, v193\n\t"
    "v_mfma_f32_16x16x32_bf16 v[200:203], a[44:47], a[140:143], v[200:203]\n\t"
    "v_exp_f32 v192, v192\n\t"
    "v_mfma_f32_16x16x32_bf16 v[204:207], a[48:51], a[128:131], v[224:227]\n\t"
    "v_cvt_pk_bf16_f32 v209, v178, v179\n\t"
    "v_mfma_f32_16x16x32_bf16 v[204:207], a[52:55], a[132:135], v[204:207]\n\t"
    "v_cvt_pk_bf16_f32 v213, v186, v187\n\t"
    "v_mfma_f32_16x16x32_bf16 v[204:207], a[56:59], a[136:139], v[204:207]\n\t"
    "v_exp_f32 v194, v194\n\t"
    "v_mfma_f32_16x16x32_bf16 v[204:207], a[60:63], a[140:143], v[204:207]\n\t"
    "v_exp_f32 v198, v198\n\t"
    "v_mfma_f32_16x16x32_bf16 v[160:163], v[208:211], a[96:99], v[160:163]\n\t"
    "v_exp_f32 v195, v195\n\t"
    "v_mfma_f32_16x16x32_bf16 v[160:163], v[212:215], a[100:103], v[160:163]\n\t"
    "ds_read_b64_tr_b16 a[96:97], v2 offset:16896\n\t"
    "v_mfma_f32_16x16x32_bf16 v[164:167], v[208:211], a[104:107], v[164:167]\n\t"
    "ds_read_b64_tr_b16 a[104:105], v3 offset:16896\n\t"
    "v_mfma_f32_16x16x32_bf16 v[164:167], v[212:215], a[108:111], v[164:167]\n\t"
    "ds_read_b64_tr_b16 a[108:109], v3 offset:25088\n\t"
    "v_mfma_f32_16x16x32_bf16 v[168:171], v[208:211], a[112:115], v[168:171]\n\t"
    "ds_read_b64_tr_b16 a[110:111], v3 offset:29184\n\t"
    "v_mfma_f32_16x16x32_bf16 v[168:171], v[212:215], a[116:119], v[168:171]\n\t"
    "v_exp_f32 v203, v203\n\t"
    "v_mfma_f32_16x16x32_bf16 v[172:175], v[208:211], a[120:123], v[172:175]\n\t"
    "ds_read_b64_tr_b16 a[102:103], v2 offset:29184\n\t"
    "v_mfma_f32_16x16x32_bf16 v[172:175], v[212:215], a[124:127], v[172:175]\n\t"
    "ds_read_b64_tr_b16 a[106:107], v3 offset:20992\n\t"
    "v_mfma_f32_16x16x32_bf16 v[144:147], v[208:211], a[64:67], v[144:147]\n\t"
    "v_exp_f32 v202, v202\n\t"
    "v_mfma_f32_16x16x32_bf16 v[144:147], v[212:215], a[68:71], v[144:147]\n\t"
    "ds_read_b64_tr_b16 a[114:115], v2 offset:21248\n\t"
    "v_mfma_f32_16x16x32_bf16 v[148:151], v[208:211], a[72:75], v[148:151]\n\t"
    "v_exp_f32 v197, v197\n\t"
    "v_mfma_f32_16x16x32_bf16 v[148:151], v[212:215], a[76:79], v[148:151]\n\t"
    "ds_read_b64_tr_b16 a[122:123], v3 offset:21248\n\t"
    "v_mfma_f32_16x16x32_bf16 v[152:155], v[208:211], a[80:83], v[152:155]\n\t"
    "v_exp_f32 v200, v200\n\t"
    "v_mfma_f32_16x16x32_bf16 v[152:155], v[212:215], a[84:87], v[152:155]\n\t"
    "v_cvt_pk_bf16_f32 v216, v192, v193\n\t"
    "v_cvt_pk_bf16_f32 v221, v202, v203\n\t"
    "v_mfma_f32_16x16x32_bf16 v[156:159], v[208:211], a[88:91], v[156:159]\n\t"
    "ds_read_b64_tr_b16 a[98:99], v2 offset:20992\n\t"
    "v_mfma_f32_16x16x32_bf16 v[156:159], v[212:215], a[92:95], v[156:159]\n\t"
    "ds_read_b64_tr_b16 a[124:125], v3 offset:25344\n\t"
    "v_smfmac_f32_16x16x64_bf16 a[228:231], a[208:211], v[208:215], v0\n\t"
    "v_mfma_f32_16x16x32_bf16 v[176:179], a[0:3], a[144:147], v[228:231]\n\t"
    "v_exp_f32 v199, v199\n\t"
    "v_mfma_f32_16x16x32_bf16 v[176:179], a[4:7], a[148:151], v[176:179]\n\t"
    "v_exp_f32 v196, v196\n\t"
    "v_mfma_f32_16x16x32_bf16 v[176:179], a[8:11], a[152:155], v[176:179]\n\t"
    "ds_read_b64_tr_b16 a[100:101], v2 offset:25088\n\t"
    "v_mfma_f32_16x16x32_bf16 v[176:179], a[12:15], a[156:159], v[176:179]\n\t"
    "v_exp_f32 v204, v204\n\t"
    "v_mfma_f32_16x16x32_bf16 v[180:183], a[16:19], a[144:147], v[228:231]\n\t"
    "ds_read_b64_tr_b16 a[126:127], v3 offset:29440\n\t"
    "v_mfma_f32_16x16x32_bf16 v[180:183], a[20:23], a[148:151], v[180:183]\n\t"
    "v_exp_f32 v205, v205\n\t"
    "v_mfma_f32_16x16x32_bf16 v[180:183], a[24:27], a[152:155], v[180:183]\n\t"
    "v_exp_f32 v207, v207\n\t"
    "v_mfma_f32_16x16x32_bf16 v[180:183], a[28:31], a[156:159], v[180:183]\n\t"
    "v_cvt_pk_bf16_f32 v218, v196, v197\n\t"
    "v_cvt_pk_bf16_f32 v222, v204, v205\n\t"
    "v_mfma_f32_16x16x32_bf16 v[184:187], a[32:35], a[144:147], v[228:231]\n\t"
    "ds_read_b64_tr_b16 a[120:121], v3 offset:17152\n\t"
    "v_mfma_f32_16x16x32_bf16 v[184:187], a[36:39], a[148:151], v[184:187]\n\t"
    "v_cvt_pk_bf16_f32 v217, v194, v195\n\t"
    "v_cvt_pk_bf16_f32 v219, v198, v199\n\t"
    "v_mfma_f32_16x16x32_bf16 v[184:187], a[40:43], a[152:155], v[184:187]\n\t"
    "v_exp_f32 v206, v206\n\t"
    "v_mfma_f32_16x16x32_bf16 v[184:187], a[44:47], a[156:159], v[184:187]\n\t"
    "v_exp_f32 v201, v201\n\t"
    "v_mfma_f32_16x16x32_bf16 v[188:191], a[48:51], a[144:147], v[228:231]\n\t"
    "v_cvt_pk_bf16_f32 v220, v200, v201\n\t"
    "v_cvt_pk_bf16_f32 v223, v206, v207\n\t"
    "v_mfma_f32_16x16x32_bf16 v[188:191], a[52:55], a[148:151], v[188:191]\n\t"
    "ds_read_b64_tr_b16 a[116:117], v2 offset:25344\n\t"
    "v_mfma_f32_16x16x32_bf16 v[188:191], a[56:59], a[152:155], v[188:191]\n\t"
    "s_waitcnt lgkmcnt(15)\n\t"
    "ds_write_b128 v14, v[216:219] offset:2048\n\t"
    "v_mfma_f32_16x16x32_bf16 v[188:191], a[60:63], a[156:159], v[188:191]\n\t"
    "v_exp_f32 v177, v177\n\t"
    "v_mfma_f32_16x16x32_bf16 v[16:19], v[216:219], a[252:255], v[16:19]\n\t"
    "ds_read_b64_tr_b16 a[112:113], v2 offset:17152\n\t"
    "v_mfma_f32_16x16x32_bf16 v[16:19], v[220:223], a[248:251], v[16:19]\n\t"
    "ds_write_b128 v14, v[220:223] offset:3072\n\t"
    "v_mfma_f32_16x16x32_bf16 v[20:23], v[216:219], a[244:247], v[20:23]\n\t"
    "v_exp_f32 v184, v184\n\t"
    "v_mfma_f32_16x16x32_bf16 v[20:23], v[220:223], a[240:243], v[20:23]\n\t"
    "ds_read_b64_tr_b16 a[118:119], v2 offset:29440\n\t"
    "v_mfma_f32_16x16x32_bf16 v[24:27], v[216:219], a[236:239], v[24:27]\n\t"
    "v_exp_f32 v176, v176\n\t"
    "v_mfma_f32_16x16x32_bf16 v[24:27], v[220:223], a[232:235], v[24:27]\n\t"
    "v_exp_f32 v185, v185\n\t"
    "v_mfma_f32_16x16x32_bf16 v[28:31], v[216:219], v[252:255], v[28:31]\n\t"
    "v_xor_b32 v2, 0x8000, v2\n\t"
    "v_xor_b32 v3, 0x8000, v3\n\t"
    "v_mfma_f32_16x16x32_bf16 v[28:31], v[220:223], v[248:251], v[28:31]\n\t"
    "v_exp_f32 v180, v180\n\t"
    "v_smfmac_f32_16x16x64_bf16 a[212:215], a[208:211], v[216:223], v0\n\t"
    "s_waitcnt vmcnt(8)\n\t"
    "s_waitcnt lgkmcnt(1)\n\t"
    "s_barrier\n\t"
    "v_mfma_f32_16x16x32_bf16 v[192:195], a[0:3], a[160:163], v[232:235]\n\t"
    "v_exp_f32 v179, v179\n\t"
    "v_mfma_f32_16x16x32_bf16 v[192:195], a[4:7], a[164:167], v[192:195]\n\t"
    "v_exp_f32 v190, v190\n\t"
    "v_mfma_f32_16x16x32_bf16 v[192:195], a[8:11], a[168:171], v[192:195]\n\t"
    "v_exp_f32 v181, v181\n\t"
    "v_mfma_f32_16x16x32_bf16 v[192:195], a[12:15], a[172:175], v[192:195]\n\t"
    "v_exp_f32 v182, v182\n\t"
    "v_mfma_f32_16x16x32_bf16 v[196:199], a[16:19], a[160:163], v[232:235]\n\t"
    "v_exp_f32 v191, v191\n\t"
    "v_mfma_f32_16x16x32_bf16 v[196:199], a[20:23], a[164:167], v[196:199]\n\t"
    "v_exp_f32 v183, v183\n\t"
    "v_mfma_f32_16x16x32_bf16 v[196:199], a[24:27], a[168:171], v[196:199]\n\t"
    "v_exp_f32 v189, v189\n\t"
    "v_mfma_f32_16x16x32_bf16 v[196:199], a[28:31], a[172:175], v[196:199]\n\t"
    "v_exp_f32 v186, v186\n\t"
    "v_mfma_f32_16x16x32_bf16 v[200:203], a[32:35], a[160:163], v[232:235]\n\t"
    "v_exp_f32 v188, v188\n\t"
    "v_mfma_f32_16x16x32_bf16 v[200:203], a[36:39], a[164:167], v[200:203]\n\t"
    "v_exp_f32 v178, v178\n\t"
    "v_mfma_f32_16x16x32_bf16 v[200:203], a[40:43], a[168:171], v[200:203]\n\t"
    "v_exp_f32 v187, v187\n\t"
    "v_mfma_f32_16x16x32_bf16 v[200:203], a[44:47], a[172:175], v[200:203]\n\t"
    "v_cvt_pk_bf16_f32 v211, v182, v183\n\t"
    "v_cvt_pk_bf16_f32 v214, v188, v189\n\t"
    "v_mfma_f32_16x16x32_bf16 v[204:207], a[48:51], a[160:163], v[232:235]\n\t"
    "v_cvt_pk_bf16_f32 v209, v178, v179\n\t"
    "v_cvt_pk_bf16_f32 v210, v180, v181\n\t"
    "v_mfma_f32_16x16x32_bf16 v[204:207], a[52:55], a[164:167], v[204:207]\n\t"
    "v_cvt_pk_bf16_f32 v208, v176, v177\n\t"
    "v_cvt_pk_bf16_f32 v213, v186, v187\n\t"
    "v_mfma_f32_16x16x32_bf16 v[204:207], a[56:59], a[168:171], v[204:207]\n\t"
    "v_cvt_pk_bf16_f32 v212, v184, v185\n\t"
    "v_cvt_pk_bf16_f32 v215, v190, v191\n\t"
    "v_mfma_f32_16x16x32_bf16 v[204:207], a[60:63], a[172:175], v[204:207]\n\t"
    "ds_read_b64_tr_b16 a[68:69], v2 offset:8192\n\t"
    "v_mfma_f32_16x16x32_bf16 v[48:51], v[208:211], a[252:255], v[48:51]\n\t"
    "ds_read_b64_tr_b16 a[64:65], v2 offset:0\n\t"
    "v_mfma_f32_16x16x32_bf16 v[48:51], v[212:215], a[248:251], v[48:51]\n\t"
    "ds_read_b64_tr_b16 a[94:95], v3 offset:12544\n\t"
    "v_mfma_f32_16x16x32_bf16 v[52:55], v[208:211], a[244:247], v[52:55]\n\t"
    "ds_read_b64_tr_b16 a[74:75], v3 offset:4096\n\t"
    "v_mfma_f32_16x16x32_bf16 v[52:55], v[212:215], a[240:243], v[52:55]\n\t"
    "ds_read_b64_tr_b16 a[80:81], v2 offset:256\n\t"
    "v_mfma_f32_16x16x32_bf16 v[56:59], v[208:211], a[236:239], v[56:59]\n\t"
    "v_exp_f32 v192, v192\n\t"
    "v_mfma_f32_16x16x32_bf16 v[56:59], v[212:215], a[232:235], v[56:59]\n\t"
    "ds_read_b64_tr_b16 a[78:79], v3 offset:12288\n\t"
    "v_mfma_f32_16x16x32_bf16 v[60:63], v[208:211], v[252:255], v[60:63]\n\t"
    "ds_read_b64_tr_b16 a[88:89], v3 offset:256\n\t"
    "v_mfma_f32_16x16x32_bf16 v[60:63], v[212:215], v[248:251], v[60:63]\n\t"
    "s_waitcnt lgkmcnt(14)\n\t"
    "v_mfma_f32_16x16x32_bf16 v[64:67], v[208:211], a[96:99], v[64:67]\n\t"
    "ds_read_b64_tr_b16 a[90:91], v3 offset:4352\n\t"
    "v_mfma_f32_16x16x32_bf16 v[64:67], v[212:215], a[100:103], v[64:67]\n\t"
    "v_exp_f32 v200, v200\n\t"
    "v_mfma_f32_16x16x32_bf16 v[68:71], v[208:211], a[104:107], v[68:71]\n\t"
    "v_exp_f32 v193, v193\n\t"
    "v_mfma_f32_16x16x32_bf16 v[68:71], v[212:215], a[108:111], v[68:71]\n\t"
    "s_waitcnt lgkmcnt(8)\n\t"
    "v_cvt_pk_bf16_f32 v216, v192, v193\n\t"
    "v_mfma_f32_16x16x32_bf16 v[72:75], v[208:211], a[112:115], v[72:75]\n\t"
    "v_exp_f32 v201, v201\n\t"
    "v_mfma_f32_16x16x32_bf16 v[72:75], v[212:215], a[116:119], v[72:75]\n\t"
    "v_exp_f32 v195, v195\n\t"
    "v_mfma_f32_16x16x32_bf16 v[76:79], v[208:211], a[120:123], v[76:79]\n\t"
    "v_exp_f32 v196, v196\n\t"
    "v_mfma_f32_16x16x32_bf16 v[76:79], v[212:215], a[124:127], v[76:79]\n\t"
    "ds_read_b64_tr_b16 a[76:77], v3 offset:8192\n\t"
    "v_smfmac_f32_16x16x64_bf16 a[216:219], a[208:211], v[208:215], v0\n\t"
    "v_mfma_f32_16x16x32_bf16 v[176:179], a[0:3], a[176:179], v[236:239]\n\t"
    "v_exp_f32 v205, v205\n\t"
    "v_mfma_f32_16x16x32_bf16 v[176:179], a[4:7], a[180:183], v[176:179]\n\t"
    "v_exp_f32 v203, v203\n\t"
    "v_mfma_f32_16x16x32_bf16 v[176:179], a[8:11], a[184:187], v[176:179]\n\t"
    "v_exp_f32 v198, v198\n\t"
    "v_mfma_f32_16x16x32_bf16 v[176:179], a[12:15], a[188:191], v[176:179]\n\t"
    "v_exp_f32 v207, v207\n\t"
    "v_mfma_f32_16x16x32_bf16 v[180:183], a[16:19], a[176:179], v[236:239]\n\t"
    "v_exp_f32 v197, v197\n\t"
    "v_mfma_f32_16x16x32_bf16 v[180:183], a[20:23], a[180:183], v[180:183]\n\t"
    "v_exp_f32 v206, v206\n\t"
    "v_mfma_f32_16x16x32_bf16 v[180:183], a[24:27], a[184:187], v[180:183]\n\t"
    "v_exp_f32 v202, v202\n\t"
    "v_mfma_f32_16x16x32_bf16 v[180:183], a[28:31], a[188:191], v[180:183]\n\t"
    "v_exp_f32 v199, v199\n\t"
    "v_mfma_f32_16x16x32_bf16 v[184:187], a[32:35], a[176:179], v[236:239]\n\t"
    "v_exp_f32 v204, v204\n\t"
    "v_mfma_f32_16x16x32_bf16 v[184:187], a[36:39], a[180:183], v[184:187]\n\t"
    "v_cvt_pk_bf16_f32 v220, v200, v201\n\t"
    "v_cvt_pk_bf16_f32 v222, v204, v205\n\t"
    "v_mfma_f32_16x16x32_bf16 v[184:187], a[40:43], a[184:187], v[184:187]\n\t"
    "v_exp_f32 v194, v194\n\t"
    "v_mfma_f32_16x16x32_bf16 v[184:187], a[44:47], a[188:191], v[184:187]\n\t"
    "v_cvt_pk_bf16_f32 v217, v194, v195\n\t"
    "v_cvt_pk_bf16_f32 v218, v196, v197\n\t"
    "v_mfma_f32_16x16x32_bf16 v[188:191], a[48:51], a[176:179], v[236:239]\n\t"
    "v_cvt_pk_bf16_f32 v219, v198, v199\n\t"
    "v_cvt_pk_bf16_f32 v223, v206, v207\n\t"
    "v_mfma_f32_16x16x32_bf16 v[188:191], a[52:55], a[180:183], v[188:191]\n\t"
    "ds_read_b64_tr_b16 a[66:67], v2 offset:4096\n\t"
    "v_mfma_f32_16x16x32_bf16 v[188:191], a[56:59], a[184:187], v[188:191]\n\t"
    "v_cvt_pk_bf16_f32 v221, v202, v203\n\t"
    "v_mfma_f32_16x16x32_bf16 v[188:191], a[60:63], a[188:191], v[188:191]\n\t"
    "ds_read_b64_tr_b16 a[82:83], v2 offset:4352\n\t"
    "v_mfma_f32_16x16x32_bf16 v[80:83], v[216:219], a[252:255], v[80:83]\n\t"
    "ds_read_b64_tr_b16 a[92:93], v3 offset:8448\n\t"
    "v_mfma_f32_16x16x32_bf16 v[80:83], v[220:223], a[248:251], v[80:83]\n\t"
    "ds_read_b64_tr_b16 a[84:85], v2 offset:8448\n\t"
    "v_mfma_f32_16x16x32_bf16 v[84:87], v[216:219], a[244:247], v[84:87]\n\t"
    "v_exp_f32 v187, v187\n\t"
    "v_mfma_f32_16x16x32_bf16 v[84:87], v[220:223], a[240:243], v[84:87]\n\t"
    "ds_read_b64_tr_b16 a[86:87], v2 offset:12544\n\t"
    "v_mfma_f32_16x16x32_bf16 v[88:91], v[216:219], a[236:239], v[88:91]\n\t"
    "ds_read_b64_tr_b16 a[72:73], v3 offset:0\n\t"
    "v_mfma_f32_16x16x32_bf16 v[88:91], v[220:223], a[232:235], v[88:91]\n\t"
    "v_exp_f32 v186, v186\n\t"
    "v_mfma_f32_16x16x32_bf16 v[92:95], v[216:219], v[252:255], v[92:95]\n\t"
    "v_exp_f32 v180, v180\n\t"
    "v_mfma_f32_16x16x32_bf16 v[92:95], v[220:223], v[248:251], v[92:95]\n\t"
    "v_cvt_pk_bf16_f32 v213, v186, v187\n\t"
    "v_xor_b32 v1, 0x8000, v1\n\t"
    "v_mfma_f32_16x16x32_bf16 v[96:99], v[216:219], a[96:99], v[96:99]\n\t"
    "v_exp_f32 v178, v178\n\t"
    "v_mfma_f32_16x16x32_bf16 v[96:99], v[220:223], a[100:103], v[96:99]\n\t"
    "v_exp_f32 v181, v181\n\t"
    "v_mfma_f32_16x16x32_bf16 v[100:103], v[216:219], a[104:107], v[100:103]\n\t"
    "v_exp_f32 v189, v189\n\t"
    "v_mfma_f32_16x16x32_bf16 v[100:103], v[220:223], a[108:111], v[100:103]\n\t"
    "ds_read_b64_tr_b16 a[70:71], v2 offset:12288\n\t"
    "v_mfma_f32_16x16x32_bf16 v[104:107], v[216:219], a[112:115], v[104:107]\n\t"
    "v_exp_f32 v191, v191\n\t"
    "v_mfma_f32_16x16x32_bf16 v[104:107], v[220:223], a[116:119], v[104:107]\n\t"
    "v_exp_f32 v190, v190\n\t"
    "v_mfma_f32_16x16x32_bf16 v[108:111], v[216:219], a[120:123], v[108:111]\n\t"
    "v_exp_f32 v176, v176\n\t"
    "v_mfma_f32_16x16x32_bf16 v[108:111], v[220:223], a[124:127], v[108:111]\n\t"
    "v_exp_f32 v177, v177\n\t"
    "v_smfmac_f32_16x16x64_bf16 a[220:223], a[208:211], v[216:223], v0\n\t"
    "v_mfma_f32_16x16x32_bf16 v[192:195], a[0:3], a[192:195], v[240:243]\n\t"
    "v_exp_f32 v183, v183\n\t"
    "v_mfma_f32_16x16x32_bf16 v[192:195], a[4:7], a[196:199], v[192:195]\n\t"
    "v_exp_f32 v188, v188\n\t"
    "v_mfma_f32_16x16x32_bf16 v[192:195], a[8:11], a[200:203], v[192:195]\n\t"
    "v_exp_f32 v182, v182\n\t"
    "v_mfma_f32_16x16x32_bf16 v[192:195], a[12:15], a[204:207], v[192:195]\n\t"
    "v_cvt_pk_bf16_f32 v208, v176, v177\n\t"
    "v_mfma_f32_16x16x32_bf16 v[196:199], a[16:19], a[192:195], v[240:243]\n\t"
    "v_exp_f32 v185, v185\n\t"
    "v_mfma_f32_16x16x32_bf16 v[196:199], a[20:23], a[196:199], v[196:199]\n\t"
    "v_cvt_pk_bf16_f32 v211, v182, v183\n\t"
    "v_mfma_f32_16x16x32_bf16 v[196:199], a[24:27], a[200:203], v[196:199]\n\t"
    "v_exp_f32 v179, v179\n\t"
    "v_mfma_f32_16x16x32_bf16 v[196:199], a[28:31], a[204:207], v[196:199]\n\t"
    "v_exp_f32 v184, v184\n\t"
    "v_mfma_f32_16x16x32_bf16 v[200:203], a[32:35], a[192:195], v[240:243]\n\t"
    "ds_read_b128 a[4:7], v1 offset:256\n\t"
    "ds_read_b128 a[12:15], v1 offset:768\n\t"
    "v_mfma_f32_16x16x32_bf16 v[200:203], a[36:39], a[196:199], v[200:203]\n\t"
    "v_cvt_pk_bf16_f32 v209, v178, v179\n\t"
    "v_mfma_f32_16x16x32_bf16 v[200:203], a[40:43], a[200:203], v[200:203]\n\t"
    "v_cvt_pk_bf16_f32 v210, v180, v181\n\t"
    "v_cvt_pk_bf16_f32 v215, v190, v191\n\t"
    "v_mfma_f32_16x16x32_bf16 v[200:203], a[44:47], a[204:207], v[200:203]\n\t"
    "v_cvt_pk_bf16_f32 v212, v184, v185\n\t"
    "v_mfma_f32_16x16x32_bf16 v[204:207], a[48:51], a[192:195], v[240:243]\n\t"
    "v_cvt_pk_bf16_f32 v214, v188, v189\n\t"
    "v_mfma_f32_16x16x32_bf16 v[204:207], a[52:55], a[196:199], v[204:207]\n\t"
    "v_exp_f32 v193, v193\n\t"
    "v_mfma_f32_16x16x32_bf16 v[204:207], a[56:59], a[200:203], v[204:207]\n\t"
    "v_exp_f32 v192, v192\n\t"
    "v_mfma_f32_16x16x32_bf16 v[204:207], a[60:63], a[204:207], v[204:207]\n\t"
    "v_exp_f32 v197, v197\n\t"
    "v_mfma_f32_16x16x32_bf16 v[112:115], v[208:211], a[252:255], v[112:115]\n\t"
    "ds_read_b128 a[0:3], v1 offset:0\n\t"
    "ds_read_b128 a[20:23], v1 offset:4352\n\t"
    "v_mfma_f32_16x16x32_bf16 v[112:115], v[212:215], a[248:251], v[112:115]\n\t"
    "v_exp_f32 v196, v196\n\t"
    "v_mfma_f32_16x16x32_bf16 v[116:119], v[208:211], a[244:247], v[116:119]\n\t"
    "ds_read_b128 a[8:11], v1 offset:512\n\t"
    "ds_read_b128 a[16:19], v1 offset:4096\n\t"
    "v_mfma_f32_16x16x32_bf16 v[116:119], v[212:215], a[240:243], v[116:119]\n\t"
    "v_exp_f32 v201, v201\n\t"
    "v_mfma_f32_16x16x32_bf16 v[120:123], v[208:211], a[236:239], v[120:123]\n\t"
    "ds_read_b128 a[24:27], v1 offset:4608\n\t"
    "ds_read_b128 a[36:39], v1 offset:8448\n\t"
    "v_mfma_f32_16x16x32_bf16 v[120:123], v[212:215], a[232:235], v[120:123]\n\t"
    "v_exp_f32 v199, v199\n\t"
    "v_mfma_f32_16x16x32_bf16 v[124:127], v[208:211], v[252:255], v[124:127]\n\t"
    "v_exp_f32 v206, v206\n\t"
    "v_mfma_f32_16x16x32_bf16 v[124:127], v[212:215], v[248:251], v[124:127]\n\t"
    "ds_read_b128 a[28:31], v1 offset:4864\n\t"
    "ds_read_b128 a[32:35], v1 offset:8192\n\t"
    "v_mfma_f32_16x16x32_bf16 v[128:131], v[208:211], a[96:99], v[128:131]\n\t"
    "v_exp_f32 v198, v198\n\t"
    "v_mfma_f32_16x16x32_bf16 v[128:131], v[212:215], a[100:103], v[128:131]\n\t"
    "ds_read_b128 a[48:51], v1 offset:12288\n\t"
    "v_mfma_f32_16x16x32_bf16 v[132:135], v[208:211], a[104:107], v[132:135]\n\t"
    "v_exp_f32 v200, v200\n\t"
    "v_mfma_f32_16x16x32_bf16 v[132:135], v[212:215], a[108:111], v[132:135]\n\t"
    "ds_read_b128 a[40:43], v1 offset:8704\n\t"
    "ds_read_b128 a[44:47], v1 offset:8960\n\t"
    "v_mfma_f32_16x16x32_bf16 v[136:139], v[208:211], a[112:115], v[136:139]\n\t"
    "v_cvt_pk_bf16_f32 v218, v196, v197\n\t"
    "v_cvt_pk_bf16_f32 v219, v198, v199\n\t"
    "v_mfma_f32_16x16x32_bf16 v[136:139], v[212:215], a[116:119], v[136:139]\n\t"
    "ds_read_b128 a[52:55], v1 offset:12544\n\t"
    "ds_read_b128 a[56:59], v1 offset:12800\n\t"
    "v_mfma_f32_16x16x32_bf16 v[140:143], v[208:211], a[120:123], v[140:143]\n\t"
    "v_cvt_pk_bf16_f32 v220, v200, v201\n\t"
    "v_mfma_f32_16x16x32_bf16 v[140:143], v[212:215], a[124:127], v[140:143]\n\t"
    "s_waitcnt lgkmcnt(6)\n\t"
    "ds_read_b128 a[60:63], v1 offset:13056\n\t"
    "v_smfmac_f32_16x16x64_bf16 a[224:227], a[208:211], v[208:215], v0\n\t"
    "v_mfma_f32_16x16x32_bf16 v[176:179], a[0:3], a[128:131], v[224:227]\n\t"
    "v_exp_f32 v205, v205\n\t"
    "v_mfma_f32_16x16x32_bf16 v[176:179], a[4:7], a[132:135], v[176:179]\n\t"
    "v_exp_f32 v195, v195\n\t"
    "v_mfma_f32_16x16x32_bf16 v[176:179], a[8:11], a[136:139], v[176:179]\n\t"
    "v_exp_f32 v194, v194\n\t"
    "v_mfma_f32_16x16x32_bf16 v[176:179], a[12:15], a[140:143], v[176:179]\n\t"
    "v_exp_f32 v202, v202\n\t"
    "v_mfma_f32_16x16x32_bf16 v[180:183], a[16:19], a[128:131], v[224:227]\n\t"
    "v_exp_f32 v207, v207\n\t"
    "v_mfma_f32_16x16x32_bf16 v[180:183], a[20:23], a[132:135], v[180:183]\n\t"
    "v_cvt_pk_bf16_f32 v223, v206, v207\n\t"
    "v_mfma_f32_16x16x32_bf16 v[180:183], a[24:27], a[136:139], v[180:183]\n\t"
    "v_exp_f32 v203, v203\n\t"
    "v_mfma_f32_16x16x32_bf16 v[180:183], a[28:31], a[140:143], v[180:183]\n\t"
    "v_cvt_pk_bf16_f32 v216, v192, v193\n\t"
    "s_waitcnt lgkmcnt(0)\n\t"
    "v_mfma_f32_16x16x32_bf16 v[184:187], a[32:35], a[128:131], v[224:227]\n\t"
    "v_exp_f32 v204, v204\n\t"
    "v_mfma_f32_16x16x32_bf16 v[184:187], a[36:39], a[132:135], v[184:187]\n\t"
    "v_cvt_pk_bf16_f32 v222, v204, v205\n\t"
    "v_mfma_f32_16x16x32_bf16 v[184:187], a[40:43], a[136:139], v[184:187]\n\t"
    "v_exp_f32 v177, v177\n\t"
    "v_mfma_f32_16x16x32_bf16 v[184:187], a[44:47], a[140:143], v[184:187]\n\t"
    "v_exp_f32 v176, v176\n\t"
    "v_mfma_f32_16x16x32_bf16 v[188:191], a[48:51], a[128:131], v[224:227]\n\t"
    "v_cvt_pk_bf16_f32 v217, v194, v195\n\t"
    "v_mfma_f32_16x16x32_bf16 v[188:191], a[52:55], a[132:135], v[188:191]\n\t"
    "v_cvt_pk_bf16_f32 v221, v202, v203\n\t"
    "v_mfma_f32_16x16x32_bf16 v[188:191], a[56:59], a[136:139], v[188:191]\n\t"
    "v_exp_f32 v178, v178\n\t"
    "v_mfma_f32_16x16x32_bf16 v[188:191], a[60:63], a[140:143], v[188:191]\n\t"
    "v_exp_f32 v182, v182\n\t"
    "v_mfma_f32_16x16x32_bf16 v[160:163], v[216:219], a[96:99], v[160:163]\n\t"
    "v_exp_f32 v179, v179\n\t"
    "v_mfma_f32_16x16x32_bf16 v[160:163], v[220:223], a[100:103], v[160:163]\n\t"
    "ds_read_b64_tr_b16 a[96:97], v2 offset:512\n\t"
    "v_mfma_f32_16x16x32_bf16 v[164:167], v[216:219], a[104:107], v[164:167]\n\t"
    "ds_read_b64_tr_b16 a[104:105], v3 offset:512\n\t"
    "v_mfma_f32_16x16x32_bf16 v[164:167], v[220:223], a[108:111], v[164:167]\n\t"
    "ds_read_b64_tr_b16 a[108:109], v3 offset:8704\n\t"
    "v_mfma_f32_16x16x32_bf16 v[168:171], v[216:219], a[112:115], v[168:171]\n\t"
    "ds_read_b64_tr_b16 a[110:111], v3 offset:12800\n\t"
    "v_mfma_f32_16x16x32_bf16 v[168:171], v[220:223], a[116:119], v[168:171]\n\t"
    "v_exp_f32 v187, v187\n\t"
    "v_mfma_f32_16x16x32_bf16 v[172:175], v[216:219], a[120:123], v[172:175]\n\t"
    "ds_read_b64_tr_b16 a[102:103], v2 offset:12800\n\t"
    "v_mfma_f32_16x16x32_bf16 v[172:175], v[220:223], a[124:127], v[172:175]\n\t"
    "ds_read_b64_tr_b16 a[106:107], v3 offset:4608\n\t"
    "v_mfma_f32_16x16x32_bf16 v[144:147], v[216:219], a[252:255], v[144:147]\n\t"
    "v_exp_f32 v186, v186\n\t"
    "v_mfma_f32_16x16x32_bf16 v[144:147], v[220:223], a[248:251], v[144:147]\n\t"
    "ds_read_b64_tr_b16 a[114:115], v2 offset:4864\n\t"
    "v_mfma_f32_16x16x32_bf16 v[148:151], v[216:219], a[244:247], v[148:151]\n\t"
    "v_exp_f32 v181, v181\n\t"
    "v_mfma_f32_16x16x32_bf16 v[148:151], v[220:223], a[240:243], v[148:151]\n\t"
    "ds_read_b64_tr_b16 a[122:123], v3 offset:4864\n\t"
    "v_mfma_f32_16x16x32_bf16 v[152:155], v[216:219], a[236:239], v[152:155]\n\t"
    "v_exp_f32 v184, v184\n\t"
    "v_mfma_f32_16x16x32_bf16 v[152:155], v[220:223], a[232:235], v[152:155]\n\t"
    "v_cvt_pk_bf16_f32 v208, v176, v177\n\t"
    "v_cvt_pk_bf16_f32 v213, v186, v187\n\t"
    "v_mfma_f32_16x16x32_bf16 v[156:159], v[216:219], v[252:255], v[156:159]\n\t"
    "ds_read_b64_tr_b16 a[98:99], v2 offset:4608\n\t"
    "v_mfma_f32_16x16x32_bf16 v[156:159], v[220:223], v[248:251], v[156:159]\n\t"
    "ds_read_b64_tr_b16 a[124:125], v3 offset:8960\n\t"
    "v_smfmac_f32_16x16x64_bf16 a[228:231], a[208:211], v[216:223], v0\n\t"
    "v_mfma_f32_16x16x32_bf16 v[192:195], a[0:3], a[144:147], v[228:231]\n\t"
    "v_exp_f32 v183, v183\n\t"
    "v_mfma_f32_16x16x32_bf16 v[192:195], a[4:7], a[148:151], v[192:195]\n\t"
    "v_exp_f32 v180, v180\n\t"
    "v_mfma_f32_16x16x32_bf16 v[192:195], a[8:11], a[152:155], v[192:195]\n\t"
    "ds_read_b64_tr_b16 a[100:101], v2 offset:8704\n\t"
    "v_mfma_f32_16x16x32_bf16 v[192:195], a[12:15], a[156:159], v[192:195]\n\t"
    "v_exp_f32 v188, v188\n\t"
    "v_mfma_f32_16x16x32_bf16 v[196:199], a[16:19], a[144:147], v[228:231]\n\t"
    "ds_read_b64_tr_b16 a[126:127], v3 offset:13056\n\t"
    "v_mfma_f32_16x16x32_bf16 v[196:199], a[20:23], a[148:151], v[196:199]\n\t"
    "v_exp_f32 v189, v189\n\t"
    "v_mfma_f32_16x16x32_bf16 v[196:199], a[24:27], a[152:155], v[196:199]\n\t"
    "v_exp_f32 v191, v191\n\t"
    "v_mfma_f32_16x16x32_bf16 v[196:199], a[28:31], a[156:159], v[196:199]\n\t"
    "v_cvt_pk_bf16_f32 v210, v180, v181\n\t"
    "v_cvt_pk_bf16_f32 v214, v188, v189\n\t"
    "v_mfma_f32_16x16x32_bf16 v[200:203], a[32:35], a[144:147], v[228:231]\n\t"
    "ds_read_b64_tr_b16 a[120:121], v3 offset:768\n\t"
    "v_mfma_f32_16x16x32_bf16 v[200:203], a[36:39], a[148:151], v[200:203]\n\t"
    "v_cvt_pk_bf16_f32 v209, v178, v179\n\t"
    "v_cvt_pk_bf16_f32 v211, v182, v183\n\t"
    "v_mfma_f32_16x16x32_bf16 v[200:203], a[40:43], a[152:155], v[200:203]\n\t"
    "v_exp_f32 v190, v190\n\t"
    "v_mfma_f32_16x16x32_bf16 v[200:203], a[44:47], a[156:159], v[200:203]\n\t"
    "v_exp_f32 v185, v185\n\t"
    "v_mfma_f32_16x16x32_bf16 v[204:207], a[48:51], a[144:147], v[228:231]\n\t"
    "v_cvt_pk_bf16_f32 v212, v184, v185\n\t"
    "v_cvt_pk_bf16_f32 v215, v190, v191\n\t"
    "v_mfma_f32_16x16x32_bf16 v[204:207], a[52:55], a[148:151], v[204:207]\n\t"
    "ds_read_b64_tr_b16 a[116:117], v2 offset:8960\n\t"
    "v_mfma_f32_16x16x32_bf16 v[204:207], a[56:59], a[152:155], v[204:207]\n\t"
    "s_waitcnt lgkmcnt(15)\n\t"
    "ds_write_b128 v14, v[208:211] offset:0\n\t"
    "v_mfma_f32_16x16x32_bf16 v[204:207], a[60:63], a[156:159], v[204:207]\n\t"
    "v_exp_f32 v193, v193\n\t"
    "v_mfma_f32_16x16x32_bf16 v[16:19], v[208:211], a[64:67], v[16:19]\n\t"
    "ds_read_b64_tr_b16 a[112:113], v2 offset:768\n\t"
    "v_mfma_f32_16x16x32_bf16 v[16:19], v[212:215], a[68:71], v[16:19]\n\t"
    "ds_write_b128 v14, v[212:215] offset:1024\n\t"
    "v_mfma_f32_16x16x32_bf16 v[20:23], v[208:211], a[72:75], v[20:23]\n\t"
    "v_exp_f32 v200, v200\n\t"
    "v_mfma_f32_16x16x32_bf16 v[20:23], v[212:215], a[76:79], v[20:23]\n\t"
    "ds_read_b64_tr_b16 a[118:119], v2 offset:13056\n\t"
    "v_mfma_f32_16x16x32_bf16 v[24:27], v[208:211], a[80:83], v[24:27]\n\t"
    "v_exp_f32 v192, v192\n\t"
    "v_mfma_f32_16x16x32_bf16 v[24:27], v[212:215], a[84:87], v[24:27]\n\t"
    "v_exp_f32 v201, v201\n\t"
    "v_mfma_f32_16x16x32_bf16 v[28:31], v[208:211], a[88:91], v[28:31]\n\t"
    "v_mfma_f32_16x16x32_bf16 v[28:31], v[212:215], a[92:95], v[28:31]\n\t"
    "v_exp_f32 v196, v196\n\t"
    "v_smfmac_f32_16x16x64_bf16 a[212:215], a[208:211], v[208:215], v0\n\t"
    "s_waitcnt vmcnt(0)\n\t"
    "s_waitcnt lgkmcnt(1)\n\t"
    "s_barrier\n\t"
    "v_mfma_f32_16x16x32_bf16 v[176:179], a[0:3], a[160:163], v[232:235]\n\t"
    "v_exp_f32 v195, v195\n\t"
    "v_mfma_f32_16x16x32_bf16 v[176:179], a[4:7], a[164:167], v[176:179]\n\t"
    "v_exp_f32 v206, v206\n\t"
    "v_mfma_f32_16x16x32_bf16 v[176:179], a[8:11], a[168:171], v[176:179]\n\t"
    "v_exp_f32 v197, v197\n\t"
    "v_mfma_f32_16x16x32_bf16 v[176:179], a[12:15], a[172:175], v[176:179]\n\t"
    "v_exp_f32 v198, v198\n\t"
    "v_mfma_f32_16x16x32_bf16 v[180:183], a[16:19], a[160:163], v[232:235]\n\t"
    "v_exp_f32 v207, v207\n\t"
    "v_mfma_f32_16x16x32_bf16 v[180:183], a[20:23], a[164:167], v[180:183]\n\t"
    "v_exp_f32 v199, v199\n\t"
    "v_mfma_f32_16x16x32_bf16 v[180:183], a[24:27], a[168:171], v[180:183]\n\t"
    "v_exp_f32 v205, v205\n\t"
    "v_mfma_f32_16x16x32_bf16 v[180:183], a[28:31], a[172:175], v[180:183]\n\t"
    "v_exp_f32 v202, v202\n\t"
    "v_mfma_f32_16x16x32_bf16 v[184:187], a[32:35], a[160:163], v[232:235]\n\t"
    "v_exp_f32 v204, v204\n\t"
    "v_mfma_f32_16x16x32_bf16 v[184:187], a[36:39], a[164:167], v[184:187]\n\t"
    "v_exp_f32 v194, v194\n\t"
    "v_mfma_f32_16x16x32_bf16 v[184:187], a[40:43], a[168:171], v[184:187]\n\t"
    "v_exp_f32 v203, v203\n\t"
    "v_mfma_f32_16x16x32_bf16 v[184:187], a[44:47], a[172:175], v[184:187]\n\t"
    "v_cvt_pk_bf16_f32 v219, v198, v199\n\t"
    "v_cvt_pk_bf16_f32 v222, v204, v205\n\t"
    "v_mfma_f32_16x16x32_bf16 v[188:191], a[48:51], a[160:163], v[232:235]\n\t"
    "v_cvt_pk_bf16_f32 v217, v194, v195\n\t"
    "v_cvt_pk_bf16_f32 v218, v196, v197\n\t"
    "v_mfma_f32_16x16x32_bf16 v[188:191], a[52:55], a[164:167], v[188:191]\n\t"
    "v_cvt_pk_bf16_f32 v216, v192, v193\n\t"
    "v_cvt_pk_bf16_f32 v221, v202, v203\n\t"
    "v_mfma_f32_16x16x32_bf16 v[188:191], a[56:59], a[168:171], v[188:191]\n\t"
    "v_cvt_pk_bf16_f32 v220, v200, v201\n\t"
    "v_cvt_pk_bf16_f32 v223, v206, v207\n\t"
    "v_mfma_f32_16x16x32_bf16 v[188:191], a[60:63], a[172:175], v[188:191]\n\t"
    "ds_read_b64_tr_b16 a[248:249], v2 offset:24576\n\t"
    "v_mfma_f32_16x16x32_bf16 v[48:51], v[216:219], a[64:67], v[48:51]\n\t"
    "ds_read_b64_tr_b16 a[252:253], v2 offset:16384\n\t"
    "v_mfma_f32_16x16x32_bf16 v[48:51], v[220:223], a[68:71], v[48:51]\n\t"
    "ds_read_b64_tr_b16 v[250:251], v3 offset:28928\n\t"
    "v_mfma_f32_16x16x32_bf16 v[52:55], v[216:219], a[72:75], v[52:55]\n\t"
    "ds_read_b64_tr_b16 a[246:247], v3 offset:20480\n\t"
    "v_mfma_f32_16x16x32_bf16 v[52:55], v[220:223], a[76:79], v[52:55]\n\t"
    "ds_read_b64_tr_b16 a[236:237], v2 offset:16640\n\t"
    "v_mfma_f32_16x16x32_bf16 v[56:59], v[216:219], a[80:83], v[56:59]\n\t"
    "v_exp_f32 v176, v176\n\t"
    "v_mfma_f32_16x16x32_bf16 v[56:59], v[220:223], a[84:87], v[56:59]\n\t"
    "ds_read_b64_tr_b16 a[242:243], v3 offset:28672\n\t"
    "v_mfma_f32_16x16x32_bf16 v[60:63], v[216:219], a[88:91], v[60:63]\n\t"
    "ds_read_b64_tr_b16 v[252:253], v3 offset:16640\n\t"
    "v_mfma_f32_16x16x32_bf16 v[60:63], v[220:223], a[92:95], v[60:63]\n\t"
    "s_waitcnt lgkmcnt(14)\n\t"
    "v_mfma_f32_16x16x32_bf16 v[64:67], v[216:219], a[96:99], v[64:67]\n\t"
    "ds_read_b64_tr_b16 v[254:255], v3 offset:20736\n\t"
    "v_mfma_f32_16x16x32_bf16 v[64:67], v[220:223], a[100:103], v[64:67]\n\t"
    "v_exp_f32 v184, v184\n\t"
    "v_mfma_f32_16x16x32_bf16 v[68:71], v[216:219], a[104:107], v[68:71]\n\t"
    "v_exp_f32 v177, v177\n\t"
    "v_mfma_f32_16x16x32_bf16 v[68:71], v[220:223], a[108:111], v[68:71]\n\t"
    "s_waitcnt lgkmcnt(8)\n\t"
    "v_cvt_pk_bf16_f32 v208, v176, v177\n\t"
    "v_mfma_f32_16x16x32_bf16 v[72:75], v[216:219], a[112:115], v[72:75]\n\t"
    "v_exp_f32 v185, v185\n\t"
    "v_mfma_f32_16x16x32_bf16 v[72:75], v[220:223], a[116:119], v[72:75]\n\t"
    "v_exp_f32 v179, v179\n\t"
    "v_mfma_f32_16x16x32_bf16 v[76:79], v[216:219], a[120:123], v[76:79]\n\t"
    "v_exp_f32 v180, v180\n\t"
    "v_mfma_f32_16x16x32_bf16 v[76:79], v[220:223], a[124:127], v[76:79]\n\t"
    "ds_read_b64_tr_b16 a[240:241], v3 offset:24576\n\t"
    "v_smfmac_f32_16x16x64_bf16 a[216:219], a[208:211], v[216:223], v0\n\t"
    "v_mfma_f32_16x16x32_bf16 v[192:195], a[0:3], a[176:179], v[236:239]\n\t"
    "v_exp_f32 v189, v189\n\t"
    "v_mfma_f32_16x16x32_bf16 v[192:195], a[4:7], a[180:183], v[192:195]\n\t"
    "v_exp_f32 v187, v187\n\t"
    "v_mfma_f32_16x16x32_bf16 v[192:195], a[8:11], a[184:187], v[192:195]\n\t"
    "v_exp_f32 v182, v182\n\t"
    "v_mfma_f32_16x16x32_bf16 v[192:195], a[12:15], a[188:191], v[192:195]\n\t"
    "v_exp_f32 v191, v191\n\t"
    "v_mfma_f32_16x16x32_bf16 v[196:199], a[16:19], a[176:179], v[236:239]\n\t"
    "v_exp_f32 v181, v181\n\t"
    "v_mfma_f32_16x16x32_bf16 v[196:199], a[20:23], a[180:183], v[196:199]\n\t"
    "v_exp_f32 v190, v190\n\t"
    "v_mfma_f32_16x16x32_bf16 v[196:199], a[24:27], a[184:187], v[196:199]\n\t"
    "v_exp_f32 v186, v186\n\t"
    "v_mfma_f32_16x16x32_bf16 v[196:199], a[28:31], a[188:191], v[196:199]\n\t"
    "v_exp_f32 v183, v183\n\t"
    "v_mfma_f32_16x16x32_bf16 v[200:203], a[32:35], a[176:179], v[236:239]\n\t"
    "v_exp_f32 v188, v188\n\t"
    "v_mfma_f32_16x16x32_bf16 v[200:203], a[36:39], a[180:183], v[200:203]\n\t"
    "v_cvt_pk_bf16_f32 v212, v184, v185\n\t"
    "v_cvt_pk_bf16_f32 v214, v188, v189\n\t"
    "v_mfma_f32_16x16x32_bf16 v[200:203], a[40:43], a[184:187], v[200:203]\n\t"
    "v_exp_f32 v178, v178\n\t"
    "v_mfma_f32_16x16x32_bf16 v[200:203], a[44:47], a[188:191], v[200:203]\n\t"
    "v_cvt_pk_bf16_f32 v209, v178, v179\n\t"
    "v_cvt_pk_bf16_f32 v210, v180, v181\n\t"
    "v_mfma_f32_16x16x32_bf16 v[204:207], a[48:51], a[176:179], v[236:239]\n\t"
    "v_cvt_pk_bf16_f32 v211, v182, v183\n\t"
    "v_cvt_pk_bf16_f32 v215, v190, v191\n\t"
    "v_mfma_f32_16x16x32_bf16 v[204:207], a[52:55], a[180:183], v[204:207]\n\t"
    "ds_read_b64_tr_b16 a[254:255], v2 offset:20480\n\t"
    "v_mfma_f32_16x16x32_bf16 v[204:207], a[56:59], a[184:187], v[204:207]\n\t"
    "v_cvt_pk_bf16_f32 v213, v186, v187\n\t"
    "v_mfma_f32_16x16x32_bf16 v[204:207], a[60:63], a[188:191], v[204:207]\n\t"
    "ds_read_b64_tr_b16 a[238:239], v2 offset:20736\n\t"
    "v_mfma_f32_16x16x32_bf16 v[80:83], v[208:211], a[64:67], v[80:83]\n\t"
    "ds_read_b64_tr_b16 v[248:249], v3 offset:24832\n\t"
    "v_mfma_f32_16x16x32_bf16 v[80:83], v[212:215], a[68:71], v[80:83]\n\t"
    "ds_read_b64_tr_b16 a[232:233], v2 offset:24832\n\t"
    "v_mfma_f32_16x16x32_bf16 v[84:87], v[208:211], a[72:75], v[84:87]\n\t"
    "v_exp_f32 v203, v203\n\t"
    "v_mfma_f32_16x16x32_bf16 v[84:87], v[212:215], a[76:79], v[84:87]\n\t"
    "ds_read_b64_tr_b16 a[234:235], v2 offset:28928\n\t"
    "v_mfma_f32_16x16x32_bf16 v[88:91], v[208:211], a[80:83], v[88:91]\n\t"
    "ds_read_b64_tr_b16 a[244:245], v3 offset:16384\n\t"
    "v_mfma_f32_16x16x32_bf16 v[88:91], v[212:215], a[84:87], v[88:91]\n\t"
    "v_exp_f32 v202, v202\n\t"
    "v_mfma_f32_16x16x32_bf16 v[92:95], v[208:211], a[88:91], v[92:95]\n\t"
    "v_exp_f32 v196, v196\n\t"
    "v_mfma_f32_16x16x32_bf16 v[92:95], v[212:215], a[92:95], v[92:95]\n\t"
    "v_cvt_pk_bf16_f32 v221, v202, v203\n\t"
    "v_mfma_f32_16x16x32_bf16 v[96:99], v[208:211], a[96:99], v[96:99]\n\t"
    "v_exp_f32 v194, v194\n\t"
    "v_mfma_f32_16x16x32_bf16 v[96:99], v[212:215], a[100:103], v[96:99]\n\t"
    "v_exp_f32 v197, v197\n\t"
    "v_mfma_f32_16x16x32_bf16 v[100:103], v[208:211], a[104:107], v[100:103]\n\t"
    "v_exp_f32 v205, v205\n\t"
    "v_mfma_f32_16x16x32_bf16 v[100:103], v[212:215], a[108:111], v[100:103]\n\t"
    "ds_read_b64_tr_b16 a[250:251], v2 offset:28672\n\t"
    "v_mfma_f32_16x16x32_bf16 v[104:107], v[208:211], a[112:115], v[104:107]\n\t"
    "v_exp_f32 v207, v207\n\t"
    "v_mfma_f32_16x16x32_bf16 v[104:107], v[212:215], a[116:119], v[104:107]\n\t"
    "v_exp_f32 v206, v206\n\t"
    "v_mfma_f32_16x16x32_bf16 v[108:111], v[208:211], a[120:123], v[108:111]\n\t"
    "v_exp_f32 v192, v192\n\t"
    "v_mfma_f32_16x16x32_bf16 v[108:111], v[212:215], a[124:127], v[108:111]\n\t"
    "v_exp_f32 v193, v193\n\t"
    "v_smfmac_f32_16x16x64_bf16 a[220:223], a[208:211], v[208:215], v0\n\t"
    "v_mfma_f32_16x16x32_bf16 v[176:179], a[0:3], a[192:195], v[240:243]\n\t"
    "v_exp_f32 v199, v199\n\t"
    "v_mfma_f32_16x16x32_bf16 v[176:179], a[4:7], a[196:199], v[176:179]\n\t"
    "v_exp_f32 v204, v204\n\t"
    "v_mfma_f32_16x16x32_bf16 v[176:179], a[8:11], a[200:203], v[176:179]\n\t"
    "v_exp_f32 v198, v198\n\t"
    "v_mfma_f32_16x16x32_bf16 v[176:179], a[12:15], a[204:207], v[176:179]\n\t"
    "v_cvt_pk_bf16_f32 v216, v192, v193\n\t"
    "v_mfma_f32_16x16x32_bf16 v[180:183], a[16:19], a[192:195], v[240:243]\n\t"
    "v_exp_f32 v201, v201\n\t"
    "v_mfma_f32_16x16x32_bf16 v[180:183], a[20:23], a[196:199], v[180:183]\n\t"
    "v_cvt_pk_bf16_f32 v219, v198, v199\n\t"
    "v_mfma_f32_16x16x32_bf16 v[180:183], a[24:27], a[200:203], v[180:183]\n\t"
    "v_exp_f32 v195, v195\n\t"
    "v_mfma_f32_16x16x32_bf16 v[180:183], a[28:31], a[204:207], v[180:183]\n\t"
    "v_exp_f32 v200, v200\n\t"
    "v_mfma_f32_16x16x32_bf16 v[184:187], a[32:35], a[192:195], v[240:243]\n\t"
    "ds_read_b128 a[4:7], v1 offset:16640\n\t"
    "ds_read_b128 a[12:15], v1 offset:17152\n\t"
    "v_mfma_f32_16x16x32_bf16 v[184:187], a[36:39], a[196:199], v[184:187]\n\t"
    "v_cvt_pk_bf16_f32 v217, v194, v195\n\t"
    "v_mfma_f32_16x16x32_bf16 v[184:187], a[40:43], a[200:203], v[184:187]\n\t"
    "v_cvt_pk_bf16_f32 v218, v196, v197\n\t"
    "v_cvt_pk_bf16_f32 v223, v206, v207\n\t"
    "v_mfma_f32_16x16x32_bf16 v[184:187], a[44:47], a[204:207], v[184:187]\n\t"
    "v_cvt_pk_bf16_f32 v220, v200, v201\n\t"
    "v_mfma_f32_16x16x32_bf16 v[188:191], a[48:51], a[192:195], v[240:243]\n\t"
    "v_cvt_pk_bf16_f32 v222, v204, v205\n\t"
    "v_mfma_f32_16x16x32_bf16 v[188:191], a[52:55], a[196:199], v[188:191]\n\t"
    "v_exp_f32 v177, v177\n\t"
    "v_mfma_f32_16x16x32_bf16 v[188:191], a[56:59], a[200:203], v[188:191]\n\t"
    "v_exp_f32 v176, v176\n\t"
    "v_mfma_f32_16x16x32_bf16 v[188:191], a[60:63], a[204:207], v[188:191]\n\t"
    "v_exp_f32 v181, v181\n\t"
    "v_mfma_f32_16x16x32_bf16 v[112:115], v[216:219], a[64:67], v[112:115]\n\t"
    "ds_read_b128 a[0:3], v1 offset:16384\n\t"
    "ds_read_b128 a[20:23], v1 offset:20736\n\t"
    "v_mfma_f32_16x16x32_bf16 v[112:115], v[220:223], a[68:71], v[112:115]\n\t"
    "v_exp_f32 v180, v180\n\t"
    "v_mfma_f32_16x16x32_bf16 v[116:119], v[216:219], a[72:75], v[116:119]\n\t"
    "ds_read_b128 a[8:11], v1 offset:16896\n\t"
    "ds_read_b128 a[16:19], v1 offset:20480\n\t"
    "v_mfma_f32_16x16x32_bf16 v[116:119], v[220:223], a[76:79], v[116:119]\n\t"
    "v_exp_f32 v185, v185\n\t"
    "v_mfma_f32_16x16x32_bf16 v[120:123], v[216:219], a[80:83], v[120:123]\n\t"
    "ds_read_b128 a[24:27], v1 offset:20992\n\t"
    "ds_read_b128 a[36:39], v1 offset:24832\n\t"
    "v_mfma_f32_16x16x32_bf16 v[120:123], v[220:223], a[84:87], v[120:123]\n\t"
    "v_exp_f32 v183, v183\n\t"
    "v_mfma_f32_16x16x32_bf16 v[124:127], v[216:219], a[88:91], v[124:127]\n\t"
    "v_exp_f32 v190, v190\n\t"
    "v_mfma_f32_16x16x32_bf16 v[124:127], v[220:223], a[92:95], v[124:127]\n\t"
    "ds_read_b128 a[28:31], v1 offset:21248\n\t"
    "ds_read_b128 a[32:35], v1 offset:24576\n\t"
    "v_mfma_f32_16x16x32_bf16 v[128:131], v[216:219], a[96:99], v[128:131]\n\t"
    "v_exp_f32 v182, v182\n\t"
    "v_mfma_f32_16x16x32_bf16 v[128:131], v[220:223], a[100:103], v[128:131]\n\t"
    "ds_read_b128 a[48:51], v1 offset:28672\n\t"
    "v_mfma_f32_16x16x32_bf16 v[132:135], v[216:219], a[104:107], v[132:135]\n\t"
    "v_exp_f32 v184, v184\n\t"
    "v_mfma_f32_16x16x32_bf16 v[132:135], v[220:223], a[108:111], v[132:135]\n\t"
    "ds_read_b128 a[40:43], v1 offset:25088\n\t"
    "ds_read_b128 a[44:47], v1 offset:25344\n\t"
    "v_mfma_f32_16x16x32_bf16 v[136:139], v[216:219], a[112:115], v[136:139]\n\t"
    "v_cvt_pk_bf16_f32 v210, v180, v181\n\t"
    "v_cvt_pk_bf16_f32 v211, v182, v183\n\t"
    "v_mfma_f32_16x16x32_bf16 v[136:139], v[220:223], a[116:119], v[136:139]\n\t"
    "ds_read_b128 a[52:55], v1 offset:28928\n\t"
    "ds_read_b128 a[56:59], v1 offset:29184\n\t"
    "v_mfma_f32_16x16x32_bf16 v[140:143], v[216:219], a[120:123], v[140:143]\n\t"
    "v_cvt_pk_bf16_f32 v212, v184, v185\n\t"
    "v_mfma_f32_16x16x32_bf16 v[140:143], v[220:223], a[124:127], v[140:143]\n\t"
    "s_waitcnt lgkmcnt(6)\n\t"
    "ds_read_b128 a[60:63], v1 offset:29440\n\t"
    "v_smfmac_f32_16x16x64_bf16 a[224:227], a[208:211], v[216:223], v0\n\t"
    "s_sub_u32 s62, s62, 1\n\t"
    "s_cbranch_scc0 LOOP5_%=\n\t"
    "v_mfma_f32_16x16x32_bf16 v[192:195], a[0:3], a[128:131], v[224:227]\n\t"
    "v_exp_f32 v189, v189\n\t"
    "v_mfma_f32_16x16x32_bf16 v[192:195], a[4:7], a[132:135], v[192:195]\n\t"
    "v_exp_f32 v179, v179\n\t"
    "v_mfma_f32_16x16x32_bf16 v[192:195], a[8:11], a[136:139], v[192:195]\n\t"
    "v_exp_f32 v178, v178\n\t"
    "v_mfma_f32_16x16x32_bf16 v[192:195], a[12:15], a[140:143], v[192:195]\n\t"
    "v_exp_f32 v186, v186\n\t"
    "v_mfma_f32_16x16x32_bf16 v[196:199], a[16:19], a[128:131], v[224:227]\n\t"
    "v_exp_f32 v191, v191\n\t"
    "v_mfma_f32_16x16x32_bf16 v[196:199], a[20:23], a[132:135], v[196:199]\n\t"
    "v_cvt_pk_bf16_f32 v215, v190, v191\n\t"
    "v_mfma_f32_16x16x32_bf16 v[196:199], a[24:27], a[136:139], v[196:199]\n\t"
    "v_exp_f32 v187, v187\n\t"
    "v_mfma_f32_16x16x32_bf16 v[196:199], a[28:31], a[140:143], v[196:199]\n\t"
    "v_cvt_pk_bf16_f32 v208, v176, v177\n\t"
    "s_waitcnt lgkmcnt(0)\n\t"
    "v_mfma_f32_16x16x32_bf16 v[200:203], a[32:35], a[128:131], v[224:227]\n\t"
    "v_exp_f32 v188, v188\n\t"
    "v_mfma_f32_16x16x32_bf16 v[200:203], a[36:39], a[132:135], v[200:203]\n\t"
    "v_cvt_pk_bf16_f32 v214, v188, v189\n\t"
    "v_mfma_f32_16x16x32_bf16 v[200:203], a[40:43], a[136:139], v[200:203]\n\t"
    "v_exp_f32 v193, v193\n\t"
    "v_mfma_f32_16x16x32_bf16 v[200:203], a[44:47], a[140:143], v[200:203]\n\t"
    "v_exp_f32 v192, v192\n\t"
    "v_mfma_f32_16x16x32_bf16 v[204:207], a[48:51], a[128:131], v[224:227]\n\t"
    "v_cvt_pk_bf16_f32 v209, v178, v179\n\t"
    "v_mfma_f32_16x16x32_bf16 v[204:207], a[52:55], a[132:135], v[204:207]\n\t"
    "v_cvt_pk_bf16_f32 v213, v186, v187\n\t"
    "v_mfma_f32_16x16x32_bf16 v[204:207], a[56:59], a[136:139], v[204:207]\n\t"
    "v_exp_f32 v194, v194\n\t"
    "v_mfma_f32_16x16x32_bf16 v[204:207], a[60:63], a[140:143], v[204:207]\n\t"
    "v_exp_f32 v198, v198\n\t"
    "v_mfma_f32_16x16x32_bf16 v[160:163], v[208:211], a[96:99], v[160:163]\n\t"
    "v_exp_f32 v195, v195\n\t"
    "v_mfma_f32_16x16x32_bf16 v[160:163], v[212:215], a[100:103], v[160:163]\n\t"
    "ds_read_b64_tr_b16 a[96:97], v2 offset:16896\n\t"
    "v_mfma_f32_16x16x32_bf16 v[164:167], v[208:211], a[104:107], v[164:167]\n\t"
    "ds_read_b64_tr_b16 a[104:105], v3 offset:16896\n\t"
    "v_mfma_f32_16x16x32_bf16 v[164:167], v[212:215], a[108:111], v[164:167]\n\t"
    "ds_read_b64_tr_b16 a[108:109], v3 offset:25088\n\t"
    "v_mfma_f32_16x16x32_bf16 v[168:171], v[208:211], a[112:115], v[168:171]\n\t"
    "ds_read_b64_tr_b16 a[110:111], v3 offset:29184\n\t"
    "v_mfma_f32_16x16x32_bf16 v[168:171], v[212:215], a[116:119], v[168:171]\n\t"
    "v_exp_f32 v203, v203\n\t"
    "v_mfma_f32_16x16x32_bf16 v[172:175], v[208:211], a[120:123], v[172:175]\n\t"
    "ds_read_b64_tr_b16 a[102:103], v2 offset:29184\n\t"
    "v_mfma_f32_16x16x32_bf16 v[172:175], v[212:215], a[124:127], v[172:175]\n\t"
    "ds_read_b64_tr_b16 a[106:107], v3 offset:20992\n\t"
    "v_mfma_f32_16x16x32_bf16 v[144:147], v[208:211], a[64:67], v[144:147]\n\t"
    "v_exp_f32 v202, v202\n\t"
    "v_mfma_f32_16x16x32_bf16 v[144:147], v[212:215], a[68:71], v[144:147]\n\t"
    "ds_read_b64_tr_b16 a[114:115], v2 offset:21248\n\t"
    "v_mfma_f32_16x16x32_bf16 v[148:151], v[208:211], a[72:75], v[148:151]\n\t"
    "v_exp_f32 v197, v197\n\t"
    "v_mfma_f32_16x16x32_bf16 v[148:151], v[212:215], a[76:79], v[148:151]\n\t"
    "ds_read_b64_tr_b16 a[122:123], v3 offset:21248\n\t"
    "v_mfma_f32_16x16x32_bf16 v[152:155], v[208:211], a[80:83], v[152:155]\n\t"
    "v_exp_f32 v200, v200\n\t"
    "v_mfma_f32_16x16x32_bf16 v[152:155], v[212:215], a[84:87], v[152:155]\n\t"
    "v_cvt_pk_bf16_f32 v216, v192, v193\n\t"
    "v_cvt_pk_bf16_f32 v221, v202, v203\n\t"
    "v_mfma_f32_16x16x32_bf16 v[156:159], v[208:211], a[88:91], v[156:159]\n\t"
    "ds_read_b64_tr_b16 a[98:99], v2 offset:20992\n\t"
    "v_mfma_f32_16x16x32_bf16 v[156:159], v[212:215], a[92:95], v[156:159]\n\t"
    "ds_read_b64_tr_b16 a[124:125], v3 offset:25344\n\t"
    "v_smfmac_f32_16x16x64_bf16 a[228:231], a[208:211], v[208:215], v0\n\t"
    "s_barrier\n\t"
    "v_mfma_f32_16x16x32_bf16 v[176:179], a[0:3], a[144:147], v[228:231]\n\t"
    "v_exp_f32 v199, v199\n\t"
    "v_mbcnt_lo_u32_b32 v11, -1, 0\n\t"
    "v_mbcnt_hi_u32_b32 v11, -1, v11\n\t"
    "v_and_b32 v9, 15, v11\n\t"
    "ds_read_b128 v[32:35], v14 offset:0\n\t"
    "v_mfma_f32_16x16x32_bf16 v[176:179], a[4:7], a[148:151], v[176:179]\n\t"
    "v_exp_f32 v196, v196\n\t"
    "v_lshlrev_b32 v8, 8, v9\n\t"
    "v_bfe_u32 v10, v11, 4, 1\n\t"
    "v_lshl_add_u32 v8, v10, 5, v8\n\t"
    "ds_read_b128 v[36:39], v14 offset:1024\n\t"
    "v_mfma_f32_16x16x32_bf16 v[176:179], a[8:11], a[152:155], v[176:179]\n\t"
    "ds_read_b64_tr_b16 a[100:101], v2 offset:25088\n\t"
    "v_lshrrev_b32 v10, 5, v11\n\t"
    "v_lshl_add_u32 v8, v10, 4, v8\n\t"
    "ds_read_b128 v[40:43], v14 offset:2048\n\t"
    "v_mfma_f32_16x16x32_bf16 v[176:179], a[12:15], a[156:159], v[176:179]\n\t"
    "v_exp_f32 v204, v204\n\t"
    "s_mul_i32 s58, %[wid], 20480\n\t"
    "s_mov_b64 s[60:61], 0\n\t"
    "s_mov_b32 s59, 0x53800000\n\t"
    "ds_read_b128 v[44:47], v14 offset:3072\n\t"
    "v_mfma_f32_16x16x32_bf16 v[180:183], a[16:19], a[144:147], v[228:231]\n\t"
    "ds_read_b64_tr_b16 a[126:127], v3 offset:29440\n\t"
    "v_mov_b32 v4, 0\n\t"
    "v_mov_b32 v5, 0\n\t"
    "v_mfma_f32_16x16x32_bf16 v[180:183], a[20:23], a[148:151], v[180:183]\n\t"
    "v_exp_f32 v205, v205\n\t"
    "v_lshrrev_b32 v10, 4, v11\n\t"
    "v_lshlrev_b32 v12, 2, v10\n\t"
    "v_xor_b32 v12, v12, v9\n\t"
    "v_mfma_f32_16x16x32_bf16 v[180:183], a[24:27], a[152:155], v[180:183]\n\t"
    "v_exp_f32 v207, v207\n\t"
    "v_lshlrev_b32 v12, 4, v12\n\t"
    "v_lshl_add_u32 v224, v10, 8, v12\n\t"
    "v_mfma_f32_16x16x32_bf16 v[180:183], a[28:31], a[156:159], v[180:183]\n\t"
    "v_cvt_pk_bf16_f32 v218, v196, v197\n\t"
    "v_cvt_pk_bf16_f32 v222, v204, v205\n\t"
    "v_add_u32 v224, s67, v224\n\t"
    "v_add_u32 v224, 0x8000, v224\n\t"
    "v_and_b32 v12, 3, v11\n\t"
    "v_mfma_f32_16x16x32_bf16 v[184:187], a[32:35], a[144:147], v[228:231]\n\t"
    "ds_read_b64_tr_b16 a[120:121], v3 offset:17152\n\t"
    "v_bfe_u32 v13, v11, 2, 2\n\t"
    "v_lshrrev_b32 v10, 5, v11\n\t"
    "v_mfma_f32_16x16x32_bf16 v[184:187], a[36:39], a[148:151], v[184:187]\n\t"
    "v_cvt_pk_bf16_f32 v217, v194, v195\n\t"
    "v_cvt_pk_bf16_f32 v219, v198, v199\n\t"
    "v_lshl_add_u32 v13, v10, 3, v13\n\t"
    "v_lshlrev_b32 v10, 2, v12\n\t"
    "v_xor_b32 v13, v13, v10\n\t"
    "v_mfma_f32_16x16x32_bf16 v[184:187], a[40:43], a[152:155], v[184:187]\n\t"
    "v_exp_f32 v206, v206\n\t"
    "v_lshlrev_b32 v13, 4, v13\n\t"
    "v_lshl_add_u32 v13, v12, 8, v13\n\t"
    "v_mfma_f32_16x16x32_bf16 v[184:187], a[44:47], a[156:159], v[184:187]\n\t"
    "v_exp_f32 v201, v201\n\t"
    "v_bfe_u32 v10, v11, 4, 1\n\t"
    "v_lshl_add_u32 v13, v10, 3, v13\n\t"
    "v_add_u32 v13, s67, v13\n\t"
    "v_mfma_f32_16x16x32_bf16 v[188:191], a[48:51], a[144:147], v[228:231]\n\t"
    "v_cvt_pk_bf16_f32 v220, v200, v201\n\t"
    "v_cvt_pk_bf16_f32 v223, v206, v207\n\t"
    "v_add_u32 v225, 0x8000, v13\n\t"
    "v_xor_b32 v226, 64, v225\n\t"
    "v_mfma_f32_16x16x32_bf16 v[188:191], a[52:55], a[148:151], v[188:191]\n\t"
    "ds_read_b64_tr_b16 a[116:117], v2 offset:25344\n\t"
    "v_mfma_f32_16x16x32_bf16 v[188:191], a[56:59], a[152:155], v[188:191]\n\t"
    "s_waitcnt lgkmcnt(15)\n\t"
    "v_mfma_f32_16x16x32_bf16 v[188:191], a[60:63], a[156:159], v[188:191]\n\t"
    "v_exp_f32 v177, v177\n\t"
    "v_mfma_f32_16x16x32_bf16 v[16:19], v[216:219], a[252:255], v[16:19]\n\t"
    "ds_read_b64_tr_b16 a[112:113], v2 offset:17152\n\t"
    "v_mfma_f32_16x16x32_bf16 v[16:19], v[220:223], a[248:251], v[16:19]\n\t"
    "v_mfma_f32_16x16x32_bf16 v[20:23], v[216:219], a[244:247], v[20:23]\n\t"
    "v_exp_f32 v184, v184\n\t"
    "v_mfma_f32_16x16x32_bf16 v[20:23], v[220:223], a[240:243], v[20:23]\n\t"
    "ds_read_b64_tr_b16 a[118:119], v2 offset:29440\n\t"
    "v_mfma_f32_16x16x32_bf16 v[24:27], v[216:219], a[236:239], v[24:27]\n\t"
    "v_exp_f32 v176, v176\n\t"
    "v_mfma_f32_16x16x32_bf16 v[24:27], v[220:223], a[232:235], v[24:27]\n\t"
    "v_exp_f32 v185, v185\n\t"
    "v_mfma_f32_16x16x32_bf16 v[28:31], v[216:219], v[252:255], v[28:31]\n\t"
    "v_mfma_f32_16x16x32_bf16 v[28:31], v[220:223], v[248:251], v[28:31]\n\t"
    "v_exp_f32 v180, v180\n\t"
    "v_smfmac_f32_16x16x64_bf16 a[212:215], a[208:211], v[216:223], v0\n\t"
    "v_mfma_f32_16x16x32_bf16 v[192:195], a[0:3], a[160:163], v[232:235]\n\t"
    "v_exp_f32 v179, v179\n\t"
    "s_waitcnt lgkmcnt(5)\n\t"
    "s_waitcnt lgkmcnt(0)\n\t"
    "v_mfma_f32_16x16x32_bf16 v[192:195], a[4:7], a[164:167], v[192:195]\n\t"
    "v_exp_f32 v190, v190\n\t"
    "v_mfma_f32_16x16x32_bf16 v[32:35], v[216:219], a[96:99], v[32:35]\n\t"
    "v_mfma_f32_16x16x32_bf16 v[192:195], a[8:11], a[168:171], v[192:195]\n\t"
    "v_exp_f32 v181, v181\n\t"
    "v_mfma_f32_16x16x32_bf16 v[32:35], v[220:223], a[100:103], v[32:35]\n\t"
    "v_mfma_f32_16x16x32_bf16 v[192:195], a[12:15], a[172:175], v[192:195]\n\t"
    "v_exp_f32 v182, v182\n\t"
    "v_mfma_f32_16x16x32_bf16 v[36:39], v[216:219], a[104:107], v[36:39]\n\t"
    "v_mfma_f32_16x16x32_bf16 v[196:199], a[16:19], a[160:163], v[232:235]\n\t"
    "v_exp_f32 v191, v191\n\t"
    "v_mfma_f32_16x16x32_bf16 v[36:39], v[220:223], a[108:111], v[36:39]\n\t"
    "v_mfma_f32_16x16x32_bf16 v[196:199], a[20:23], a[164:167], v[196:199]\n\t"
    "v_exp_f32 v183, v183\n\t"
    "v_mfma_f32_16x16x32_bf16 v[40:43], v[216:219], a[112:115], v[40:43]\n\t"
    "v_accvgpr_read_b32 v10, a212\n\t"
    "v_accvgpr_read_b32 v11, a213\n\t"
    "v_add_f32 v10, v10, v11\n\t"
    "v_mfma_f32_16x16x32_bf16 v[196:199], a[24:27], a[168:171], v[196:199]\n\t"
    "v_exp_f32 v189, v189\n\t"
    "v_mfma_f32_16x16x32_bf16 v[40:43], v[220:223], a[116:119], v[40:43]\n\t"
    "v_cmp_nge_f32 vcc, s59, v10\n\t"
    "v_rcp_f32 v10, v10\n\t"
    "s_or_b64 s[60:61], s[60:61], vcc\n\t"
    "v_mfma_f32_16x16x32_bf16 v[196:199], a[28:31], a[172:175], v[196:199]\n\t"
    "v_exp_f32 v186, v186\n\t"
    "v_mfma_f32_16x16x32_bf16 v[44:47], v[216:219], a[120:123], v[44:47]\n\t"
    "v_mov_b32 v11, v10\n\t"
    "v_mov_b32_dpp v11, v10 row_shl:4 row_mask:0x2 bank_mask:0xf\n\t"
    "v_mov_b32_dpp v11, v10 row_shl:8 row_mask:0x4 bank_mask:0xf\n\t"
    "v_mfma_f32_16x16x32_bf16 v[200:203], a[32:35], a[160:163], v[232:235]\n\t"
    "v_exp_f32 v188, v188\n\t"
    "v_mfma_f32_16x16x32_bf16 v[44:47], v[220:223], a[124:127], v[44:47]\n\t"
    "v_mov_b32_dpp v11, v10 row_shl:12 row_mask:0x8 bank_mask:0xf\n\t"
    "s_nop 1\n\t"
    "v_mul_f32_dpp v16, v11, v16 row_newbcast:0 row_mask:0xf bank_mask:0xf\n\t"
    "v_mul_f32_dpp v17, v11, v17 row_newbcast:1 row_mask:0xf bank_mask:0xf\n\t"
    "v_mfma_f32_16x16x32_bf16 v[200:203], a[36:39], a[164:167], v[200:203]\n\t"
    "v_exp_f32 v178, v178\n\t"
    "v_mul_f32_dpp v18, v11, v18 row_newbcast:2 row_mask:0xf bank_mask:0xf\n\t"
    "v_mul_f32_dpp v19, v11, v19 row_newbcast:3 row_mask:0xf bank_mask:0xf\n\t"
    "v_mfma_f32_16x16x32_bf16 v[200:203], a[40:43], a[168:171], v[200:203]\n\t"
    "v_exp_f32 v187, v187\n\t"
    "v_mul_f32_dpp v20, v11, v20 row_newbcast:0 row_mask:0xf bank_mask:0xf\n\t"
    "v_mul_f32_dpp v21, v11, v21 row_newbcast:1 row_mask:0xf bank_mask:0xf\n\t"
    "v_mul_f32_dpp v22, v11, v22 row_newbcast:2 row_mask:0xf bank_mask:0xf\n\t"
    "v_mfma_f32_16x16x32_bf16 v[200:203], a[44:47], a[172:175], v[200:203]\n\t"
    "v_cvt_pk_bf16_f32 v211, v182, v183\n\t"
    "v_cvt_pk_bf16_f32 v214, v188, v189\n\t"
    "v_mul_f32_dpp v23, v11, v23 row_newbcast:3 row_mask:0xf bank_mask:0xf\n\t"
    "v_cvt_pk_bf16_f32 v23, v22, v23\n\t"
    "v_cvt_pk_bf16_f32 v22, v20, v21\n\t"
    "v_mfma_f32_16x16x32_bf16 v[204:207], a[48:51], a[160:163], v[232:235]\n\t"
    "v_cvt_pk_bf16_f32 v209, v178, v179\n\t"
    "v_cvt_pk_bf16_f32 v210, v180, v181\n\t"
    "v_cvt_pk_bf16_f32 v21, v18, v19\n\t"
    "v_cvt_pk_bf16_f32 v20, v16, v17\n\t"
    "ds_write_b128 v224, v[20:23] offset:0\n\t"
    "v_mfma_f32_16x16x32_bf16 v[204:207], a[52:55], a[164:167], v[204:207]\n\t"
    "v_cvt_pk_bf16_f32 v208, v176, v177\n\t"
    "v_cvt_pk_bf16_f32 v213, v186, v187\n\t"
    "ds_read_b64_tr_b16 v[16:17], v225 offset:0\n\t"
    "ds_read_b64_tr_b16 v[18:19], v226 offset:0\n\t"
    "v_mul_f32_dpp v24, v11, v24 row_newbcast:0 row_mask:0xf bank_mask:0xf\n\t"
    "v_mfma_f32_16x16x32_bf16 v[204:207], a[56:59], a[168:171], v[204:207]\n\t"
    "v_cvt_pk_bf16_f32 v212, v184, v185\n\t"
    "v_cvt_pk_bf16_f32 v215, v190, v191\n\t"
    "v_mul_f32_dpp v25, v11, v25 row_newbcast:1 row_mask:0xf bank_mask:0xf\n\t"
    "v_mul_f32_dpp v26, v11, v26 row_newbcast:2 row_mask:0xf bank_mask:0xf\n\t"
    "v_mfma_f32_16x16x32_bf16 v[204:207], a[60:63], a[172:175], v[204:207]\n\t"
    "v_mul_f32_dpp v27, v11, v27 row_newbcast:3 row_mask:0xf bank_mask:0xf\n\t"
    "v_mul_f32_dpp v28, v11, v28 row_newbcast:0 row_mask:0xf bank_mask:0xf\n\t"
    "v_mul_f32_dpp v29, v11, v29 row_newbcast:1 row_mask:0xf bank_mask:0xf\n\t"
    "v_mfma_f32_16x16x32_bf16 v[48:51], v[208:211], a[252:255], v[48:51]\n\t"
    "v_mul_f32_dpp v30, v11, v30 row_newbcast:2 row_mask:0xf bank_mask:0xf\n\t"
    "v_mul_f32_dpp v31, v11, v31 row_newbcast:3 row_mask:0xf bank_mask:0xf\n\t"
    "v_cvt_pk_bf16_f32 v24, v24, v25\n\t"
    "v_mfma_f32_16x16x32_bf16 v[48:51], v[212:215], a[248:251], v[48:51]\n\t"
    "v_cvt_pk_bf16_f32 v25, v26, v27\n\t"
    "v_cvt_pk_bf16_f32 v26, v28, v29\n\t"
    "v_cvt_pk_bf16_f32 v27, v30, v31\n\t"
    "v_mfma_f32_16x16x32_bf16 v[52:55], v[208:211], a[244:247], v[52:55]\n\t"
    "ds_write_b128 v224, v[24:27] offset:1024\n\t"
    "ds_read_b64_tr_b16 v[28:29], v225 offset:1024\n\t"
    "v_mfma_f32_16x16x32_bf16 v[52:55], v[212:215], a[240:243], v[52:55]\n\t"
    "ds_read_b64_tr_b16 v[30:31], v226 offset:1024\n\t"
    "v_smfmac_f32_16x16x64_bf16 v[4:7], a[208:211], v[20:27], v0\n\t"
    "s_waitcnt lgkmcnt(3)\n\t"
    "v_mfma_f32_16x16x32_bf16 v[56:59], v[208:211], a[236:239], v[56:59]\n\t"
    "v_exp_f32 v192, v192\n\t"
    "buffer_store_dwordx4 v[16:19], v8, s[52:55], s58 offen sc0 sc1\n\t"
    "v_mul_f32_dpp v32, v11, v32 row_newbcast:0 row_mask:0xf bank_mask:0xf\n\t"
    "v_mul_f32_dpp v33, v11, v33 row_newbcast:1 row_mask:0xf bank_mask:0xf\n\t"
    "v_mfma_f32_16x16x32_bf16 v[56:59], v[212:215], a[232:235], v[56:59]\n\t"
    "v_mul_f32_dpp v34, v11, v34 row_newbcast:2 row_mask:0xf bank_mask:0xf\n\t"
    "v_mul_f32_dpp v35, v11, v35 row_newbcast:3 row_mask:0xf bank_mask:0xf\n\t"
    "v_mul_f32_dpp v36, v11, v36 row_newbcast:0 row_mask:0xf bank_mask:0xf\n\t"
    "v_mfma_f32_16x16x32_bf16 v[60:63], v[208:211], v[252:255], v[60:63]\n\t"
    "v_mul_f32_dpp v37, v11, v37 row_newbcast:1 row_mask:0xf bank_mask:0xf\n\t"
    "v_mul_f32_dpp v38, v11, v38 row_newbcast:2 row_mask:0xf bank_mask:0xf\n\t"
    "v_mul_f32_dpp v39, v11, v39 row_newbcast:3 row_mask:0xf bank_mask:0xf\n\t"
    "v_mfma_f32_16x16x32_bf16 v[60:63], v[212:215], v[248:251], v[60:63]\n\t"
    "s_waitcnt lgkmcnt(13)\n\t"
    "v_cvt_pk_bf16_f32 v39, v38, v39\n\t"
    "v_cvt_pk_bf16_f32 v38, v36, v37\n\t"
    "v_mfma_f32_16x16x32_bf16 v[64:67], v[208:211], a[96:99], v[64:67]\n\t"
    "v_cvt_pk_bf16_f32 v37, v34, v35\n\t"
    "v_cvt_pk_bf16_f32 v36, v32, v33\n\t"
    "ds_write_b128 v224, v[36:39] offset:2048\n\t"
    "v_mfma_f32_16x16x32_bf16 v[64:67], v[212:215], a[100:103], v[64:67]\n\t"
    "v_exp_f32 v200, v200\n\t"
    "ds_read_b64_tr_b16 v[32:33], v225 offset:2048\n\t"
    "ds_read_b64_tr_b16 v[34:35], v226 offset:2048\n\t"
    "s_waitcnt lgkmcnt(3)\n\t"
    "v_mfma_f32_16x16x32_bf16 v[68:71], v[208:211], a[104:107], v[68:71]\n\t"
    "v_exp_f32 v193, v193\n\t"
    "buffer_store_dwordx4 v[28:31], v8, s[52:55], s58 offen offset:64 sc0 sc1\n\t"
    "v_mul_f32_dpp v40, v11, v40 row_newbcast:0 row_mask:0xf bank_mask:0xf\n\t"
    "v_mul_f32_dpp v41, v11, v41 row_newbcast:1 row_mask:0xf bank_mask:0xf\n\t"
    "v_mfma_f32_16x16x32_bf16 v[68:71], v[212:215], a[108:111], v[68:71]\n\t"
    "s_waitcnt lgkmcnt(9)\n\t"
    "v_cvt_pk_bf16_f32 v216, v192, v193\n\t"
    "v_mul_f32_dpp v42, v11, v42 row_newbcast:2 row_mask:0xf bank_mask:0xf\n\t"
    "v_mul_f32_dpp v43, v11, v43 row_newbcast:3 row_mask:0xf bank_mask:0xf\n\t"
    "v_mul_f32_dpp v44, v11, v44 row_newbcast:0 row_mask:0xf bank_mask:0xf\n\t"
    "v_mfma_f32_16x16x32_bf16 v[72:75], v[208:211], a[112:115], v[72:75]\n\t"
    "v_exp_f32 v201, v201\n\t"
    "v_mul_f32_dpp v45, v11, v45 row_newbcast:1 row_mask:0xf bank_mask:0xf\n\t"
    "v_mul_f32_dpp v46, v11, v46 row_newbcast:2 row_mask:0xf bank_mask:0xf\n\t"
    "v_mfma_f32_16x16x32_bf16 v[72:75], v[212:215], a[116:119], v[72:75]\n\t"
    "v_exp_f32 v195, v195\n\t"
    "v_mul_f32_dpp v47, v11, v47 row_newbcast:3 row_mask:0xf bank_mask:0xf\n\t"
    "v_cvt_pk_bf16_f32 v40, v40, v41\n\t"
    "v_cvt_pk_bf16_f32 v41, v42, v43\n\t"
    "v_mfma_f32_16x16x32_bf16 v[76:79], v[208:211], a[120:123], v[76:79]\n\t"
    "v_exp_f32 v196, v196\n\t"
    "v_cvt_pk_bf16_f32 v42, v44, v45\n\t"
    "v_cvt_pk_bf16_f32 v43, v46, v47\n\t"
    "ds_write_b128 v224, v[40:43] offset:3072\n\t"
    "v_mfma_f32_16x16x32_bf16 v[76:79], v[212:215], a[124:127], v[76:79]\n\t"
    "ds_read_b64_tr_b16 v[44:45], v225 offset:3072\n\t"
    "ds_read_b64_tr_b16 v[46:47], v226 offset:3072\n\t"
    "v_smfmac_f32_16x16x64_bf16 v[4:7], a[208:211], v[36:43], v0\n\t"
    "v_smfmac_f32_16x16x64_bf16 a[216:219], a[208:211], v[208:215], v0\n\t"
    "s_waitcnt lgkmcnt(3)\n\t"
    "buffer_store_dwordx4 v[32:35], v8, s[52:55], s58 offen offset:128 sc0 sc1\n\t"
    "v_mfma_f32_16x16x32_bf16 v[176:179], a[0:3], a[176:179], v[236:239]\n\t"
    "v_exp_f32 v205, v205\n\t"
    "v_mfma_f32_16x16x32_bf16 v[176:179], a[4:7], a[180:183], v[176:179]\n\t"
    "v_exp_f32 v203, v203\n\t"
    "v_mfma_f32_16x16x32_bf16 v[176:179], a[8:11], a[184:187], v[176:179]\n\t"
    "v_exp_f32 v198, v198\n\t"
    "v_mfma_f32_16x16x32_bf16 v[176:179], a[12:15], a[188:191], v[176:179]\n\t"
    "v_exp_f32 v207, v207\n\t"
    "v_mfma_f32_16x16x32_bf16 v[180:183], a[16:19], a[176:179], v[236:239]\n\t"
    "v_exp_f32 v197, v197\n\t"
    "v_mfma_f32_16x16x32_bf16 v[180:183], a[20:23], a[180:183], v[180:183]\n\t"
    "v_exp_f32 v206, v206\n\t"
    "v_accvgpr_read_b32 v12, a216\n\t"
    "v_accvgpr_read_b32 v13, a217\n\t"
    "v_add_f32 v12, v12, v13\n\t"
    "v_mfma_f32_16x16x32_bf16 v[180:183], a[24:27], a[184:187], v[180:183]\n\t"
    "v_exp_f32 v202, v202\n\t"
    "v_cmp_nge_f32 vcc, s59, v12\n\t"
    "v_rcp_f32 v12, v12\n\t"
    "v_mfma_f32_16x16x32_bf16 v[180:183], a[28:31], a[188:191], v[180:183]\n\t"
    "v_exp_f32 v199, v199\n\t"
    "s_or_b64 s[60:61], s[60:61], vcc\n\t"
    "v_mov_b32 v13, v12\n\t"
    "v_mov_b32_dpp v13, v12 row_shl:4 row_mask:0x2 bank_mask:0xf\n\t"
    "v_mfma_f32_16x16x32_bf16 v[184:187], a[32:35], a[176:179], v[236:239]\n\t"
    "v_exp_f32 v204, v204\n\t"
    "v_mov_b32_dpp v13, v12 row_shl:8 row_mask:0x4 bank_mask:0xf\n\t"
    "v_mov_b32_dpp v13, v12 row_shl:12 row_mask:0x8 bank_mask:0xf\n\t"
    "v_mfma_f32_16x16x32_bf16 v[184:187], a[36:39], a[180:183], v[184:187]\n\t"
    "v_cvt_pk_bf16_f32 v220, v200, v201\n\t"
    "v_cvt_pk_bf16_f32 v222, v204, v205\n\t"
    "s_waitcnt lgkmcnt(0)\n\t"
    "buffer_store_dwordx4 v[44:47], v8, s[52:55], s58 offen offset:192 sc0 sc1\n\t"
    "s_add_u32 s58, s58, 4096\n\t"
    "v_mfma_f32_16x16x32_bf16 v[184:187], a[40:43], a[184:187], v[184:187]\n\t"
    "v_exp_f32 v194, v194\n\t"
    "v_mov_b32_dpp v228, v13 row_newbcast:0 row_mask:0xf bank_mask:0xf\n\t"
    "v_mov_b32_dpp v229, v13 row_newbcast:1 row_mask:0xf bank_mask:0xf\n\t"
    "v_mfma_f32_16x16x32_bf16 v[184:187], a[44:47], a[188:191], v[184:187]\n\t"
    "v_cvt_pk_bf16_f32 v217, v194, v195\n\t"
    "v_cvt_pk_bf16_f32 v218, v196, v197\n\t"
    "v_mov_b32_dpp v230, v13 row_newbcast:2 row_mask:0xf bank_mask:0xf\n\t"
    "v_mov_b32_dpp v231, v13 row_newbcast:3 row_mask:0xf bank_mask:0xf\n\t"
    "v_pk_mul_f32 v[48:49], v[48:49], v[228:229]\n\t"
    "v_mfma_f32_16x16x32_bf16 v[188:191], a[48:51], a[176:179], v[236:239]\n\t"
    "v_cvt_pk_bf16_f32 v219, v198, v199\n\t"
    "v_cvt_pk_bf16_f32 v223, v206, v207\n\t"
    "v_pk_mul_f32 v[50:51], v[50:51], v[230:231]\n\t"
    "v_pk_mul_f32 v[52:53], v[52:53], v[228:229]\n\t"
    "v_mfma_f32_16x16x32_bf16 v[188:191], a[52:55], a[180:183], v[188:191]\n\t"
    "v_pk_mul_f32 v[54:55], v[54:55], v[230:231]\n\t"
    "v_cvt_pk_bf16_f32 v55, v54, v55\n\t"
    "v_cvt_pk_bf16_f32 v54, v52, v53\n\t"
    "v_mfma_f32_16x16x32_bf16 v[188:191], a[56:59], a[184:187], v[188:191]\n\t"
    "v_cvt_pk_bf16_f32 v221, v202, v203\n\t"
    "v_cvt_pk_bf16_f32 v53, v50, v51\n\t"
    "v_cvt_pk_bf16_f32 v52, v48, v49\n\t"
    "v_mfma_f32_16x16x32_bf16 v[188:191], a[60:63], a[188:191], v[188:191]\n\t"
    "ds_write_b128 v224, v[52:55] offset:0\n\t"
    "ds_read_b64_tr_b16 v[48:49], v225 offset:0\n\t"
    "ds_read_b64_tr_b16 v[50:51], v226 offset:0\n\t"
    "v_mfma_f32_16x16x32_bf16 v[80:83], v[216:219], a[252:255], v[80:83]\n\t"
    "v_pk_mul_f32 v[56:57], v[56:57], v[228:229]\n\t"
    "v_pk_mul_f32 v[58:59], v[58:59], v[230:231]\n\t"
    "v_mfma_f32_16x16x32_bf16 v[80:83], v[220:223], a[248:251], v[80:83]\n\t"
    "v_pk_mul_f32 v[60:61], v[60:61], v[228:229]\n\t"
    "v_pk_mul_f32 v[62:63], v[62:63], v[230:231]\n\t"
    "v_cvt_pk_bf16_f32 v56, v56, v57\n\t"
    "v_mfma_f32_16x16x32_bf16 v[84:87], v[216:219], a[244:247], v[84:87]\n\t"
    "v_exp_f32 v187, v187\n\t"
    "v_cvt_pk_bf16_f32 v57, v58, v59\n\t"
    "v_cvt_pk_bf16_f32 v58, v60, v61\n\t"
    "v_mfma_f32_16x16x32_bf16 v[84:87], v[220:223], a[240:243], v[84:87]\n\t"
    "v_cvt_pk_bf16_f32 v59, v62, v63\n\t"
    "ds_write_b128 v224, v[56:59] offset:1024\n\t"
    "v_mfma_f32_16x16x32_bf16 v[88:91], v[216:219], a[236:239], v[88:91]\n\t"
    "ds_read_b64_tr_b16 v[60:61], v225 offset:1024\n\t"
    "ds_read_b64_tr_b16 v[62:63], v226 offset:1024\n\t"
    "v_smfmac_f32_16x16x64_bf16 v[4:7], a[208:211], v[52:59], v0\n\t"
    "v_mfma_f32_16x16x32_bf16 v[88:91], v[220:223], a[232:235], v[88:91]\n\t"
    "v_exp_f32 v186, v186\n\t"
    "s_waitcnt lgkmcnt(3)\n\t"
    "buffer_store_dwordx4 v[48:51], v8, s[52:55], s58 offen sc0 sc1\n\t"
    "v_mfma_f32_16x16x32_bf16 v[92:95], v[216:219], v[252:255], v[92:95]\n\t"
    "v_exp_f32 v180, v180\n\t"
    "v_pk_mul_f32 v[64:65], v[64:65], v[228:229]\n\t"
    "v_pk_mul_f32 v[66:67], v[66:67], v[230:231]\n\t"
    "v_pk_mul_f32 v[68:69], v[68:69], v[228:229]\n\t"
    "v_mfma_f32_16x16x32_bf16 v[92:95], v[220:223], v[248:251], v[92:95]\n\t"
    "v_cvt_pk_bf16_f32 v213, v186, v187\n\t"
    "v_pk_mul_f32 v[70:71], v[70:71], v[230:231]\n\t"
    "v_cvt_pk_bf16_f32 v71, v70, v71\n\t"
    "v_mfma_f32_16x16x32_bf16 v[96:99], v[216:219], a[96:99], v[96:99]\n\t"
    "v_exp_f32 v178, v178\n\t"
    "v_cvt_pk_bf16_f32 v70, v68, v69\n\t"
    "v_cvt_pk_bf16_f32 v69, v66, v67\n\t"
    "v_cvt_pk_bf16_f32 v68, v64, v65\n\t"
    "v_mfma_f32_16x16x32_bf16 v[96:99], v[220:223], a[100:103], v[96:99]\n\t"
    "v_exp_f32 v181, v181\n\t"
    "ds_write_b128 v224, v[68:71] offset:2048\n\t"
    "ds_read_b64_tr_b16 v[64:65], v225 offset:2048\n\t"
    "v_mfma_f32_16x16x32_bf16 v[100:103], v[216:219], a[104:107], v[100:103]\n\t"
    "v_exp_f32 v189, v189\n\t"
    "ds_read_b64_tr_b16 v[66:67], v226 offset:2048\n\t"
    "s_waitcnt lgkmcnt(3)\n\t"
    "buffer_store_dwordx4 v[60:63], v8, s[52:55], s58 offen offset:64 sc0 sc1\n\t"
    "v_mfma_f32_16x16x32_bf16 v[100:103], v[220:223], a[108:111], v[100:103]\n\t"
    "v_pk_mul_f32 v[72:73], v[72:73], v[228:229]\n\t"
    "v_pk_mul_f32 v[74:75], v[74:75], v[230:231]\n\t"
    "v_mfma_f32_16x16x32_bf16 v[104:107], v[216:219], a[112:115], v[104:107]\n\t"
    "v_exp_f32 v191, v191\n\t"
    "v_pk_mul_f32 v[76:77], v[76:77], v[228:229]\n\t"
    "v_pk_mul_f32 v[78:79], v[78:79], v[230:231]\n\t"
    "v_cvt_pk_bf16_f32 v72, v72, v73\n\t"
    "v_mfma_f32_16x16x32_bf16 v[104:107], v[220:223], a[116:119], v[104:107]\n\t"
    "v_exp_f32 v190, v190\n\t"
    "v_cvt_pk_bf16_f32 v73, v74, v75\n\t"
    "v_cvt_pk_bf16_f32 v74, v76, v77\n\t"
    "v_mfma_f32_16x16x32_bf16 v[108:111], v[216:219], a[120:123], v[108:111]\n\t"
    "v_exp_f32 v176, v176\n\t"
    "v_cvt_pk_bf16_f32 v75, v78, v79\n\t"
    "ds_write_b128 v224, v[72:75] offset:3072\n\t"
    "ds_read_b64_tr_b16 v[76:77], v225 offset:3072\n\t"
    "v_mfma_f32_16x16x32_bf16 v[108:111], v[220:223], a[124:127], v[108:111]\n\t"
    "v_exp_f32 v177, v177\n\t"
    "ds_read_b64_tr_b16 v[78:79], v226 offset:3072\n\t"
    "v_smfmac_f32_16x16x64_bf16 v[4:7], a[208:211], v[68:75], v0\n\t"
    "v_smfmac_f32_16x16x64_bf16 a[220:223], a[208:211], v[216:223], v0\n\t"
    "s_waitcnt lgkmcnt(3)\n\t"
    "buffer_store_dwordx4 v[64:67], v8, s[52:55], s58 offen offset:128 sc0 sc1\n\t"
    "v_mfma_f32_16x16x32_bf16 v[192:195], a[0:3], a[192:195], v[240:243]\n\t"
    "v_exp_f32 v183, v183\n\t"
    "v_mfma_f32_16x16x32_bf16 v[192:195], a[4:7], a[196:199], v[192:195]\n\t"
    "v_exp_f32 v188, v188\n\t"
    "v_mfma_f32_16x16x32_bf16 v[192:195], a[8:11], a[200:203], v[192:195]\n\t"
    "v_exp_f32 v182, v182\n\t"
    "v_mfma_f32_16x16x32_bf16 v[192:195], a[12:15], a[204:207], v[192:195]\n\t"
    "v_cvt_pk_bf16_f32 v208, v176, v177\n\t"
    "v_mfma_f32_16x16x32_bf16 v[196:199], a[16:19], a[192:195], v[240:243]\n\t"
    "v_exp_f32 v185, v185\n\t"
    "v_mfma_f32_16x16x32_bf16 v[196:199], a[20:23], a[196:199], v[196:199]\n\t"
    "v_cvt_pk_bf16_f32 v211, v182, v183\n\t"
    "v_accvgpr_read_b32 v10, a220\n\t"
    "v_accvgpr_read_b32 v11, a221\n\t"
    "v_add_f32 v10, v10, v11\n\t"
    "v_mfma_f32_16x16x32_bf16 v[196:199], a[24:27], a[200:203], v[196:199]\n\t"
    "v_exp_f32 v179, v179\n\t"
    "v_cmp_nge_f32 vcc, s59, v10\n\t"
    "v_rcp_f32 v10, v10\n\t"
    "v_mfma_f32_16x16x32_bf16 v[196:199], a[28:31], a[204:207], v[196:199]\n\t"
    "v_exp_f32 v184, v184\n\t"
    "s_or_b64 s[60:61], s[60:61], vcc\n\t"
    "v_mov_b32 v11, v10\n\t"
    "v_mov_b32_dpp v11, v10 row_shl:4 row_mask:0x2 bank_mask:0xf\n\t"
    "v_mfma_f32_16x16x32_bf16 v[200:203], a[32:35], a[192:195], v[240:243]\n\t"
    "v_mov_b32_dpp v11, v10 row_shl:8 row_mask:0x4 bank_mask:0xf\n\t"
    "v_mov_b32_dpp v11, v10 row_shl:12 row_mask:0x8 bank_mask:0xf\n\t"
    "v_mfma_f32_16x16x32_bf16 v[200:203], a[36:39], a[196:199], v[200:203]\n\t"
    "v_cvt_pk_bf16_f32 v209, v178, v179\n\t"
    "s_waitcnt lgkmcnt(0)\n\t"
    "buffer_store_dwordx4 v[76:79], v8, s[52:55], s58 offen offset:192 sc0 sc1\n\t"
    "s_add_u32 s58, s58, 4096\n\t"
    "v_mfma_f32_16x16x32_bf16 v[200:203], a[40:43], a[200:203], v[200:203]\n\t"
    "v_cvt_pk_bf16_f32 v210, v180, v181\n\t"
    "v_cvt_pk_bf16_f32 v215, v190, v191\n\t"
    "v_mov_b32_dpp v232, v11 row_newbcast:0 row_mask:0xf bank_mask:0xf\n\t"
    "v_mov_b32_dpp v233, v11 row_newbcast:1 row_mask:0xf bank_mask:0xf\n\t"
    "v_mfma_f32_16x16x32_bf16 v[200:203], a[44:47], a[204:207], v[200:203]\n\t"
    "v_cvt_pk_bf16_f32 v212, v184, v185\n\t"
    "v_mov_b32_dpp v234, v11 row_newbcast:2 row_mask:0xf bank_mask:0xf\n\t"
    "v_mov_b32_dpp v235, v11 row_newbcast:3 row_mask:0xf bank_mask:0xf\n\t"
    "v_pk_mul_f32 v[80:81], v[80:81], v[232:233]\n\t"
    "v_mfma_f32_16x16x32_bf16 v[204:207], a[48:51], a[192:195], v[240:243]\n\t"
    "v_cvt_pk_bf16_f32 v214, v188, v189\n\t"
    "v_pk_mul_f32 v[82:83], v[82:83], v[234:235]\n\t"
    "v_pk_mul_f32 v[84:85], v[84:85], v[232:233]\n\t"
    "v_mfma_f32_16x16x32_bf16 v[204:207], a[52:55], a[196:199], v[204:207]\n\t"
    "v_exp_f32 v193, v193\n\t"
    "v_pk_mul_f32 v[86:87], v[86:87], v[234:235]\n\t"
    "v_cvt_pk_bf16_f32 v87, v86, v87\n\t"
    "v_cvt_pk_bf16_f32 v86, v84, v85\n\t"
    "v_mfma_f32_16x16x32_bf16 v[204:207], a[56:59], a[200:203], v[204:207]\n\t"
    "v_exp_f32 v192, v192\n\t"
    "v_cvt_pk_bf16_f32 v85, v82, v83\n\t"
    "v_cvt_pk_bf16_f32 v84, v80, v81\n\t"
    "v_mfma_f32_16x16x32_bf16 v[204:207], a[60:63], a[204:207], v[204:207]\n\t"
    "v_exp_f32 v197, v197\n\t"
    "ds_write_b128 v224, v[84:87] offset:0\n\t"
    "ds_read_b64_tr_b16 v[80:81], v225 offset:0\n\t"
    "ds_read_b64_tr_b16 v[82:83], v226 offset:0\n\t"
    "v_mfma_f32_16x16x32_bf16 v[112:115], v[208:211], a[252:255], v[112:115]\n\t"
    "v_pk_mul_f32 v[88:89], v[88:89], v[232:233]\n\t"
    "v_pk_mul_f32 v[90:91], v[90:91], v[234:235]\n\t"
    "v_mfma_f32_16x16x32_bf16 v[112:115], v[212:215], a[248:251], v[112:115]\n\t"
    "v_exp_f32 v196, v196\n\t"
    "v_pk_mul_f32 v[92:93], v[92:93], v[232:233]\n\t"
    "v_pk_mul_f32 v[94:95], v[94:95], v[234:235]\n\t"
    "v_cvt_pk_bf16_f32 v88, v88, v89\n\t"
    "v_mfma_f32_16x16x32_bf16 v[116:119], v[208:211], a[244:247], v[116:119]\n\t"
    "v_cvt_pk_bf16_f32 v89, v90, v91\n\t"
    "v_cvt_pk_bf16_f32 v90, v92, v93\n\t"
    "v_mfma_f32_16x16x32_bf16 v[116:119], v[212:215], a[240:243], v[116:119]\n\t"
    "v_exp_f32 v201, v201\n\t"
    "v_cvt_pk_bf16_f32 v91, v94, v95\n\t"
    "ds_write_b128 v224, v[88:91] offset:1024\n\t"
    "v_mfma_f32_16x16x32_bf16 v[120:123], v[208:211], a[236:239], v[120:123]\n\t"
    "ds_read_b64_tr_b16 v[92:93], v225 offset:1024\n\t"
    "ds_read_b64_tr_b16 v[94:95], v226 offset:1024\n\t"
    "v_smfmac_f32_16x16x64_bf16 v[4:7], a[208:211], v[84:91], v0\n\t"
    "v_mfma_f32_16x16x32_bf16 v[120:123], v[212:215], a[232:235], v[120:123]\n\t"
    "v_exp_f32 v199, v199\n\t"
    "s_waitcnt lgkmcnt(3)\n\t"
    "buffer_store_dwordx4 v[80:83], v8, s[52:55], s58 offen sc0 sc1\n\t"
    "v_mfma_f32_16x16x32_bf16 v[124:127], v[208:211], v[252:255], v[124:127]\n\t"
    "v_exp_f32 v206, v206\n\t"
    "v_pk_mul_f32 v[96:97], v[96:97], v[232:233]\n\t"
    "v_pk_mul_f32 v[98:99], v[98:99], v[234:235]\n\t"
    "v_pk_mul_f32 v[100:101], v[100:101], v[232:233]\n\t"
    "v_mfma_f32_16x16x32_bf16 v[124:127], v[212:215], v[248:251], v[124:127]\n\t"
    "v_pk_mul_f32 v[102:103], v[102:103], v[234:235]\n\t"
    "v_cvt_pk_bf16_f32 v103, v102, v103\n\t"
    "v_mfma_f32_16x16x32_bf16 v[128:131], v[208:211], a[96:99], v[128:131]\n\t"
    "v_exp_f32 v198, v198\n\t"
    "v_cvt_pk_bf16_f32 v102, v100, v101\n\t"
    "v_cvt_pk_bf16_f32 v101, v98, v99\n\t"
    "v_cvt_pk_bf16_f32 v100, v96, v97\n\t"
    "v_mfma_f32_16x16x32_bf16 v[128:131], v[212:215], a[100:103], v[128:131]\n\t"
    "ds_write_b128 v224, v[100:103] offset:2048\n\t"
    "ds_read_b64_tr_b16 v[96:97], v225 offset:2048\n\t"
    "v_mfma_f32_16x16x32_bf16 v[132:135], v[208:211], a[104:107], v[132:135]\n\t"
    "v_exp_f32 v200, v200\n\t"
    "ds_read_b64_tr_b16 v[98:99], v226 offset:2048\n\t"
    "s_waitcnt lgkmcnt(3)\n\t"
    "buffer_store_dwordx4 v[92:95], v8, s[52:55], s58 offen offset:64 sc0 sc1\n\t"
    "v_mfma_f32_16x16x32_bf16 v[132:135], v[212:215], a[108:111], v[132:135]\n\t"
    "v_pk_mul_f32 v[104:105], v[104:105], v[232:233]\n\t"
    "v_pk_mul_f32 v[106:107], v[106:107], v[234:235]\n\t"
    "v_mfma_f32_16x16x32_bf16 v[136:139], v[208:211], a[112:115], v[136:139]\n\t"
    "v_cvt_pk_bf16_f32 v218, v196, v197\n\t"
    "v_cvt_pk_bf16_f32 v219, v198, v199\n\t"
    "v_pk_mul_f32 v[108:109], v[108:109], v[232:233]\n\t"
    "v_pk_mul_f32 v[110:111], v[110:111], v[234:235]\n\t"
    "v_cvt_pk_bf16_f32 v104, v104, v105\n\t"
    "v_mfma_f32_16x16x32_bf16 v[136:139], v[212:215], a[116:119], v[136:139]\n\t"
    "v_cvt_pk_bf16_f32 v105, v106, v107\n\t"
    "v_cvt_pk_bf16_f32 v106, v108, v109\n\t"
    "v_mfma_f32_16x16x32_bf16 v[140:143], v[208:211], a[120:123], v[140:143]\n\t"
    "v_cvt_pk_bf16_f32 v220, v200, v201\n\t"
    "v_cvt_pk_bf16_f32 v107, v110, v111\n\t"
    "ds_write_b128 v224, v[104:107] offset:3072\n\t"
    "ds_read_b64_tr_b16 v[108:109], v225 offset:3072\n\t"
    "v_mfma_f32_16x16x32_bf16 v[140:143], v[212:215], a[124:127], v[140:143]\n\t"
    "ds_read_b64_tr_b16 v[110:111], v226 offset:3072\n\t"
    "v_smfmac_f32_16x16x64_bf16 v[4:7], a[208:211], v[100:107], v0\n\t"
    "v_smfmac_f32_16x16x64_bf16 a[224:227], a[208:211], v[208:215], v0\n\t"
    "s_waitcnt lgkmcnt(3)\n\t"
    "buffer_store_dwordx4 v[96:99], v8, s[52:55], s58 offen offset:128 sc0 sc1\n\t"
    "v_exp_f32 v205, v205\n\t"
    "v_exp_f32 v195, v195\n\t"
    "v_exp_f32 v194, v194\n\t"
    "v_exp_f32 v202, v202\n\t"
    "v_exp_f32 v207, v207\n\t"
    "s_nop 0\n\t"
    "v_cvt_pk_bf16_f32 v223, v206, v207\n\t"
    "v_exp_f32 v203, v203\n\t"
    "v_cvt_pk_bf16_f32 v216, v192, v193\n\t"
    "v_exp_f32 v204, v204\n\t"
    "s_nop 0\n\t"
    "v_cvt_pk_bf16_f32 v222, v204, v205\n\t"
    "v_cvt_pk_bf16_f32 v217, v194, v195\n\t"
    "v_cvt_pk_bf16_f32 v221, v202, v203\n\t"
    "s_nop 0\n\t"
    "v_mfma_f32_16x16x32_bf16 v[160:163], v[216:219], a[96:99], v[160:163]\n\t"
    "v_accvgpr_read_b32 v12, a224\n\t"
    "v_accvgpr_read_b32 v13, a225\n\t"
    "v_add_f32 v12, v12, v13\n\t"
    "v_cmp_nge_f32 vcc, s59, v12\n\t"
    "v_rcp_f32 v12, v12\n\t"
    "v_mfma_f32_16x16x32_bf16 v[160:163], v[220:223], a[100:103], v[160:163]\n\t"
    "s_or_b64 s[60:61], s[60:61], vcc\n\t"
    "v_mov_b32 v13, v12\n\t"
    "v_mov_b32_dpp v13, v12 row_shl:4 row_mask:0x2 bank_mask:0xf\n\t"
    "v_mov_b32_dpp v13, v12 row_shl:8 row_mask:0x4 bank_mask:0xf\n\t"
    "v_mfma_f32_16x16x32_bf16 v[164:167], v[216:219], a[104:107], v[164:167]\n\t"
    "v_mov_b32_dpp v13, v12 row_shl:12 row_mask:0x8 bank_mask:0xf\n\t"
    "s_waitcnt lgkmcnt(0)\n\t"
    "buffer_store_dwordx4 v[108:111], v8, s[52:55], s58 offen offset:192 sc0 sc1\n\t"
    "s_add_u32 s58, s58, 4096\n\t"
    "v_mfma_f32_16x16x32_bf16 v[164:167], v[220:223], a[108:111], v[164:167]\n\t"
    "v_mov_b32_dpp v236, v13 row_newbcast:0 row_mask:0xf bank_mask:0xf\n\t"
    "v_mov_b32_dpp v237, v13 row_newbcast:1 row_mask:0xf bank_mask:0xf\n\t"
    "v_mov_b32_dpp v238, v13 row_newbcast:2 row_mask:0xf bank_mask:0xf\n\t"
    "v_mov_b32_dpp v239, v13 row_newbcast:3 row_mask:0xf bank_mask:0xf\n\t"
    "v_mfma_f32_16x16x32_bf16 v[168:171], v[216:219], a[112:115], v[168:171]\n\t"
    "v_pk_mul_f32 v[112:113], v[112:113], v[236:237]\n\t"
    "v_pk_mul_f32 v[114:115], v[114:115], v[238:239]\n\t"
    "v_pk_mul_f32 v[116:117], v[116:117], v[236:237]\n\t"
    "v_pk_mul_f32 v[118:119], v[118:119], v[238:239]\n\t"
    "v_mfma_f32_16x16x32_bf16 v[168:171], v[220:223], a[116:119], v[168:171]\n\t"
    "v_cvt_pk_bf16_f32 v119, v118, v119\n\t"
    "v_cvt_pk_bf16_f32 v118, v116, v117\n\t"
    "v_cvt_pk_bf16_f32 v117, v114, v115\n\t"
    "v_cvt_pk_bf16_f32 v116, v112, v113\n\t"
    "v_mfma_f32_16x16x32_bf16 v[172:175], v[216:219], a[120:123], v[172:175]\n\t"
    "ds_write_b128 v224, v[116:119] offset:0\n\t"
    "ds_read_b64_tr_b16 v[112:113], v225 offset:0\n\t"
    "ds_read_b64_tr_b16 v[114:115], v226 offset:0\n\t"
    "v_pk_mul_f32 v[120:121], v[120:121], v[236:237]\n\t"
    "v_mfma_f32_16x16x32_bf16 v[172:175], v[220:223], a[124:127], v[172:175]\n\t"
    "v_pk_mul_f32 v[122:123], v[122:123], v[238:239]\n\t"
    "v_pk_mul_f32 v[124:125], v[124:125], v[236:237]\n\t"
    "v_pk_mul_f32 v[126:127], v[126:127], v[238:239]\n\t"
    "v_cvt_pk_bf16_f32 v120, v120, v121\n\t"
    "v_mfma_f32_16x16x32_bf16 v[144:147], v[216:219], a[252:255], v[144:147]\n\t"
    "v_cvt_pk_bf16_f32 v121, v122, v123\n\t"
    "v_cvt_pk_bf16_f32 v122, v124, v125\n\t"
    "v_cvt_pk_bf16_f32 v123, v126, v127\n\t"
    "ds_write_b128 v224, v[120:123] offset:1024\n\t"
    "v_mfma_f32_16x16x32_bf16 v[144:147], v[220:223], a[248:251], v[144:147]\n\t"
    "ds_read_b64_tr_b16 v[124:125], v225 offset:1024\n\t"
    "ds_read_b64_tr_b16 v[126:127], v226 offset:1024\n\t"
    "v_smfmac_f32_16x16x64_bf16 v[4:7], a[208:211], v[116:123], v0\n\t"
    "s_waitcnt lgkmcnt(3)\n\t"
    "v_mfma_f32_16x16x32_bf16 v[148:151], v[216:219], a[244:247], v[148:151]\n\t"
    "buffer_store_dwordx4 v[112:115], v8, s[52:55], s58 offen sc0 sc1\n\t"
    "v_pk_mul_f32 v[128:129], v[128:129], v[236:237]\n\t"
    "v_pk_mul_f32 v[130:131], v[130:131], v[238:239]\n\t"
    "v_pk_mul_f32 v[132:133], v[132:133], v[236:237]\n\t"
    "v_mfma_f32_16x16x32_bf16 v[148:151], v[220:223], a[240:243], v[148:151]\n\t"
    "v_pk_mul_f32 v[134:135], v[134:135], v[238:239]\n\t"
    "v_cvt_pk_bf16_f32 v135, v134, v135\n\t"
    "v_cvt_pk_bf16_f32 v134, v132, v133\n\t"
    "v_cvt_pk_bf16_f32 v133, v130, v131\n\t"
    "v_mfma_f32_16x16x32_bf16 v[152:155], v[216:219], a[236:239], v[152:155]\n\t"
    "v_cvt_pk_bf16_f32 v132, v128, v129\n\t"
    "ds_write_b128 v224, v[132:135] offset:2048\n\t"
    "ds_read_b64_tr_b16 v[128:129], v225 offset:2048\n\t"
    "ds_read_b64_tr_b16 v[130:131], v226 offset:2048\n\t"
    "v_mfma_f32_16x16x32_bf16 v[152:155], v[220:223], a[232:235], v[152:155]\n\t"
    "s_waitcnt lgkmcnt(3)\n\t"
    "buffer_store_dwordx4 v[124:127], v8, s[52:55], s58 offen offset:64 sc0 sc1\n\t"
    "v_pk_mul_f32 v[136:137], v[136:137], v[236:237]\n\t"
    "v_pk_mul_f32 v[138:139], v[138:139], v[238:239]\n\t"
    "v_mfma_f32_16x16x32_bf16 v[156:159], v[216:219], v[252:255], v[156:159]\n\t"
    "v_pk_mul_f32 v[140:141], v[140:141], v[236:237]\n\t"
    "v_pk_mul_f32 v[142:143], v[142:143], v[238:239]\n\t"
    "v_cvt_pk_bf16_f32 v136, v136, v137\n\t"
    "v_cvt_pk_bf16_f32 v137, v138, v139\n\t"
    "v_mfma_f32_16x16x32_bf16 v[156:159], v[220:223], v[248:251], v[156:159]\n\t"
    "v_cvt_pk_bf16_f32 v138, v140, v141\n\t"
    "v_cvt_pk_bf16_f32 v139, v142, v143\n\t"
    "ds_write_b128 v224, v[136:139] offset:3072\n\t"
    "ds_read_b64_tr_b16 v[140:141], v225 offset:3072\n\t"
    "v_smfmac_f32_16x16x64_bf16 a[228:231], a[208:211], v[216:223], v0\n\t"
    "ds_read_b64_tr_b16 v[142:143], v226 offset:3072\n\t"
    "v_smfmac_f32_16x16x64_bf16 v[4:7], a[208:211], v[132:139], v0\n\t"
    "s_waitcnt lgkmcnt(3)\n\t"
    "buffer_store_dwordx4 v[128:131], v8, s[52:55], s58 offen offset:128 sc0 sc1\n\t"
    "s_nop 3\n\t"
    "v_accvgpr_read_b32 v10, a228\n\t"
    "v_accvgpr_read_b32 v11, a229\n\t"
    "v_add_f32 v10, v10, v11\n\t"
    "v_cmp_nge_f32 vcc, s59, v10\n\t"
    "v_rcp_f32 v10, v10\n\t"
    "s_or_b64 s[60:61], s[60:61], vcc\n\t"
    "v_mov_b32 v11, v10\n\t"
    "v_mov_b32_dpp v11, v10 row_shl:4 row_mask:0x2 bank_mask:0xf\n\t"
    "v_mov_b32_dpp v11, v10 row_shl:8 row_mask:0x4 bank_mask:0xf\n\t"
    "v_mov_b32_dpp v11, v10 row_shl:12 row_mask:0x8 bank_mask:0xf\n\t"
    "s_waitcnt lgkmcnt(0)\n\t"
    "buffer_store_dwordx4 v[140:143], v8, s[52:55], s58 offen offset:192 sc0 sc1\n\t"
    "s_add_u32 s58, s58, 4096\n\t"
    "v_mov_b32_dpp v240, v11 row_newbcast:0 row_mask:0xf bank_mask:0xf\n\t"
    "v_mov_b32_dpp v241, v11 row_newbcast:1 row_mask:0xf bank_mask:0xf\n\t"
    "v_mov_b32_dpp v242, v11 row_newbcast:2 row_mask:0xf bank_mask:0xf\n\t"
    "v_mov_b32_dpp v243, v11 row_newbcast:3 row_mask:0xf bank_mask:0xf\n\t"
    "v_pk_mul_f32 v[144:145], v[144:145], v[240:241]\n\t"
    "v_pk_mul_f32 v[146:147], v[146:147], v[242:243]\n\t"
    "v_pk_mul_f32 v[148:149], v[148:149], v[240:241]\n\t"
    "v_pk_mul_f32 v[150:151], v[150:151], v[242:243]\n\t"
    "v_cvt_pk_bf16_f32 v151, v150, v151\n\t"
    "v_cvt_pk_bf16_f32 v150, v148, v149\n\t"
    "v_cvt_pk_bf16_f32 v149, v146, v147\n\t"
    "v_cvt_pk_bf16_f32 v148, v144, v145\n\t"
    "ds_write_b128 v224, v[148:151] offset:0\n\t"
    "ds_read_b64_tr_b16 v[144:145], v225 offset:0\n\t"
    "ds_read_b64_tr_b16 v[146:147], v226 offset:0\n\t"
    "v_pk_mul_f32 v[152:153], v[152:153], v[240:241]\n\t"
    "v_pk_mul_f32 v[154:155], v[154:155], v[242:243]\n\t"
    "v_pk_mul_f32 v[156:157], v[156:157], v[240:241]\n\t"
    "v_pk_mul_f32 v[158:159], v[158:159], v[242:243]\n\t"
    "v_cvt_pk_bf16_f32 v152, v152, v153\n\t"
    "v_cvt_pk_bf16_f32 v153, v154, v155\n\t"
    "v_cvt_pk_bf16_f32 v154, v156, v157\n\t"
    "v_cvt_pk_bf16_f32 v155, v158, v159\n\t"
    "ds_write_b128 v224, v[152:155] offset:1024\n\t"
    "ds_read_b64_tr_b16 v[156:157], v225 offset:1024\n\t"
    "ds_read_b64_tr_b16 v[158:159], v226 offset:1024\n\t"
    "v_smfmac_f32_16x16x64_bf16 v[4:7], a[208:211], v[148:155], v0\n\t"
    "s_waitcnt lgkmcnt(3)\n\t"
    "buffer_store_dwordx4 v[144:147], v8, s[52:55], s58 offen sc0 sc1\n\t"
    "v_pk_mul_f32 v[160:161], v[160:161], v[240:241]\n\t"
    "v_pk_mul_f32 v[162:163], v[162:163], v[242:243]\n\t"
    "v_pk_mul_f32 v[164:165], v[164:165], v[240:241]\n\t"
    "v_pk_mul_f32 v[166:167], v[166:167], v[242:243]\n\t"
    "v_cvt_pk_bf16_f32 v167, v166, v167\n\t"
    "v_cvt_pk_bf16_f32 v166, v164, v165\n\t"
    "v_cvt_pk_bf16_f32 v165, v162, v163\n\t"
    "v_cvt_pk_bf16_f32 v164, v160, v161\n\t"
    "ds_write_b128 v224, v[164:167] offset:2048\n\t"
    "ds_read_b64_tr_b16 v[160:161], v225 offset:2048\n\t"
    "ds_read_b64_tr_b16 v[162:163], v226 offset:2048\n\t"
    "s_waitcnt lgkmcnt(3)\n\t"
    "buffer_store_dwordx4 v[156:159], v8, s[52:55], s58 offen offset:64 sc0 sc1\n\t"
    "v_pk_mul_f32 v[168:169], v[168:169], v[240:241]\n\t"
    "v_pk_mul_f32 v[170:171], v[170:171], v[242:243]\n\t"
    "v_pk_mul_f32 v[172:173], v[172:173], v[240:241]\n\t"
    "v_pk_mul_f32 v[174:175], v[174:175], v[242:243]\n\t"
    "v_cvt_pk_bf16_f32 v168, v168, v169\n\t"
    "v_cvt_pk_bf16_f32 v169, v170, v171\n\t"
    "v_cvt_pk_bf16_f32 v170, v172, v173\n\t"
    "v_cvt_pk_bf16_f32 v171, v174, v175\n\t"
    "ds_write_b128 v224, v[168:171] offset:3072\n\t"
    "ds_read_b64_tr_b16 v[172:173], v225 offset:3072\n\t"
    "ds_read_b64_tr_b16 v[174:175], v226 offset:3072\n\t"
    "v_smfmac_f32_16x16x64_bf16 v[4:7], a[208:211], v[164:171], v0\n\t"
    "s_waitcnt lgkmcnt(3)\n\t"
    "buffer_store_dwordx4 v[160:163], v8, s[52:55], s58 offen offset:128 sc0 sc1\n\t"
    "s_waitcnt lgkmcnt(0)\n\t"
    "buffer_store_dwordx4 v[172:175], v8, s[52:55], s58 offen offset:192 sc0 sc1\n\t"
    "s_nop 3\n\t"
    "v_add_f32 v4, v4, v5\n\t"
    "v_mov_b32 v9, 0x207\n\t"
    "v_cmp_class_f32 vcc, v4, v9\n\t"
    "s_nop 1\n\t"
    "s_or_b64 s[60:61], s[60:61], vcc\n\t"
    "s_cmp_lg_u64 s[60:61], 0\n\t"
    "s_cbranch_scc1 DONE_%=\n\t"
    "s_endpgm\n\t"
    "PATH4_%=:\n\t"
    "v_lshrrev_b32 v11, 2, v9\n\t"
    "v_lshlrev_b32 v12, 10, v11\n\t"
    "v_and_b32 v13, 3, v9\n\t"
    "v_lshl_add_u32 v12, v13, 6, v12\n\t"
    "v_lshlrev_b32 v11, 1, v11\n\t"
    "v_mov_b32 v13, 0x78\n\t"
    "v_lshrrev_b32 v11, v11, v13\n\t"
    "v_and_b32 v11, 3, v11\n\t"
    "v_xor_b32 v11, v11, v10\n\t"
    "v_lshl_add_u32 v1, v11, 4, v12\n\t"
    "v_add_u32 v1, %[lds], v1\n\t"
    "v_lshlrev_b32 v12, 10, v10\n\t"
    "v_bfe_u32 v13, v0, 2, 2\n\t"
    "v_lshl_add_u32 v12, v13, 6, v12\n\t"
    "v_and_b32 v13, 3, v0\n\t"
    "v_lshl_add_u32 v12, v13, 3, v12\n\t"
    "v_and_b32 v13, 1, v10\n\t"
    "v_lshl_add_u32 v12, v13, 5, v12\n\t"
    "v_add_u32 v2, %[lds], v12\n\t"
    "v_add_u32 v2, 0x10000, v2\n\t"
    "v_xor_b32 v3, 32, v2\n\t"
    "v_mov_b32 v112, 0\n\t"
    "v_mov_b32 v113, 0\n\t"
    "v_mov_b32 v114, 0\n\t"
    "v_mov_b32 v115, 0\n\t"
    "v_mov_b32 v116, 0\n\t"
    "v_mov_b32 v117, 0\n\t"
    "v_mov_b32 v118, 0\n\t"
    "v_mov_b32 v119, 0\n\t"
    "v_mov_b32 v120, 0\n\t"
    "v_mov_b32 v121, 0\n\t"
    "v_mov_b32 v122, 0\n\t"
    "v_mov_b32 v123, 0\n\t"
    "v_mov_b32 v124, 0\n\t"
    "v_mov_b32 v125, 0\n\t"
    "v_mov_b32 v126, 0\n\t"
    "v_mov_b32 v127, 0\n\t"
    "v_mov_b32 v128, 0\n\t"
    "v_mov_b32 v129, 0\n\t"
    "v_mov_b32 v130, 0\n\t"
    "v_mov_b32 v131, 0\n\t"
    "v_mov_b32 v132, 0\n\t"
    "v_mov_b32 v133, 0\n\t"
    "v_mov_b32 v134, 0\n\t"
    "v_mov_b32 v135, 0\n\t"
    "v_mov_b32 v136, 0\n\t"
    "v_mov_b32 v137, 0\n\t"
    "v_mov_b32 v138, 0\n\t"
    "v_mov_b32 v139, 0\n\t"
    "v_mov_b32 v140, 0\n\t"
    "v_mov_b32 v141, 0\n\t"
    "v_mov_b32 v142, 0\n\t"
    "v_mov_b32 v143, 0\n\t"
    "v_lshrrev_b32 v11, 2, v9\n\t"
    "v_and_b32 v12, 1, v0\n\t"
    "v_lshlrev_b32 v12, 4, v12\n\t"
    "v_and_b32 v13, 2, v0\n\t"
    "v_cmp_eq_u32 vcc, v11, v10\n\t"
    "v_mov_b32 v14, 0x3e03\n\t"
    "v_lshlrev_b32 v14, v12, v14\n\t"
    "v_cndmask_b32 v14, 0, v14, vcc\n\t"
    "v_mov_b32 v16, v14\n\t"
    "v_mov_b32 v14, 0xb9d8\n\t"
    "v_lshlrev_b32 v14, v12, v14\n\t"
    "v_cndmask_b32 v14, 0, v14, vcc\n\t"
    "v_mov_b32 v17, v14\n\t"
    "v_cmp_eq_u32 vcc, 0, v13\n\t"
    "v_cndmask_b32 v14, 0, v16, vcc\n\t"
    "v_xor_b32 v15, v14, v16\n\t"
    "v_accvgpr_write_b32 a234, v14\n\t"
    "v_accvgpr_write_b32 a235, v15\n\t"
    "v_cndmask_b32 v14, 0, v17, vcc\n\t"
    "v_xor_b32 v15, v14, v17\n\t"
    "v_accvgpr_write_b32 a238, v14\n\t"
    "v_accvgpr_write_b32 a239, v15\n\t"
    "v_accvgpr_write_b32 a232, 0\n\t"
    "v_accvgpr_write_b32 a233, 0\n\t"
    "v_accvgpr_write_b32 a236, 0\n\t"
    "v_accvgpr_write_b32 a237, 0\n\t"
    "v_accvgpr_write_b32 a240, 0\n\t"
    "v_accvgpr_write_b32 a241, 0\n\t"
    "v_accvgpr_write_b32 a212, 0\n\t"
    "v_accvgpr_write_b32 a213, 0\n\t"
    "v_accvgpr_write_b32 a214, 0\n\t"
    "v_accvgpr_write_b32 a215, 0\n\t"
    "v_accvgpr_write_b32 a216, 0\n\t"
    "v_accvgpr_write_b32 a217, 0\n\t"
    "v_accvgpr_write_b32 a218, 0\n\t"
    "v_accvgpr_write_b32 a219, 0\n\t"
    "v_accvgpr_write_b32 a220, 0\n\t"
    "v_accvgpr_write_b32 a221, 0\n\t"
    "v_accvgpr_write_b32 a222, 0\n\t"
    "v_accvgpr_write_b32 a223, 0\n\t"
    "v_accvgpr_write_b32 a224, 0\n\t"
    "v_accvgpr_write_b32 a225, 0\n\t"
    "v_accvgpr_write_b32 a226, 0\n\t"
    "v_accvgpr_write_b32 a227, 0\n\t"
    "v_mov_b32 v12, 0x3f803f80\n\t"
    "v_accvgpr_write_b32 a208, v12\n\t"
    "v_accvgpr_write_b32 a209, v12\n\t"
    "v_accvgpr_write_b32 a210, v12\n\t"
    "v_accvgpr_write_b32 a211, v12\n\t"
    "s_mov_b32 s60, 0x3e0293ee\n\t"
    "s_mov_b32 s61, 0x3e0293ee\n\t"
    "v_lshlrev_b32 v14, 4, v0\n\t"
    "v_add_u32 v14, %[lds], v14\n\t"
    "v_add_u32 v14, 0x21000, v14\n\t"
    "v_and_b32 v11, 1, v0\n\t"
    "v_mov_b32 v12, 0x44444444\n\t"
    "v_mov_b32 v13, 0xeeeeeeee\n\t"
    "v_cmp_eq_u32 vcc, 1, v11\n\t"
    "v_cndmask_b32 v0, v12, v13, vcc\n\t"
    "v_add_u32 v9, s66, v1\n\t"
    "v_add_u32 v8, 0xc000, v9\n\t"
    "v_add_u32 v9, s66, v9\n\t"
    "v_add_u32 v9, 0x20000, v9\n\t"
    "s_add_u32 m0, s67, s66\n\t"
    "s_add_u32 m0, m0, 135168\n\t"
    "s_nop 0\n\t"
    "buffer_load_dwordx4 v4, s[48:51], s58 offen nt lds\n\t"
    "buffer_load_dwordx4 v5, s[48:51], s58 offen offset:1024 nt lds\n\t"
    "buffer_load_dwordx4 v6, s[48:51], s58 offen offset:2048 nt lds\n\t"
    "buffer_load_dwordx4 v7, s[48:51], s58 offen offset:3072 nt lds\n\t"
    "s_add_u32 s58, s58, 4096\n\t"
    "s_waitcnt vmcnt(8)\n\t"
    "ds_read_b128 a[128:131], v9 offset:0\n\t"
    "ds_read_b128 a[132:135], v9 offset:256\n\t"
    "ds_read_b128 a[136:139], v9 offset:512\n\t"
    "ds_read_b128 a[140:143], v9 offset:768\n\t"
    "s_barrier\n\t"
    "ds_read_b128 a[0:3], v1 offset:0\n\t"
    "ds_read_b128 a[4:7], v1 offset:256\n\t"
    "ds_read_b128 a[8:11], v1 offset:512\n\t"
    "ds_read_b128 a[12:15], v1 offset:768\n\t"
    "ds_read_b128 a[16:19], v1 offset:4096\n\t"
    "ds_read_b128 a[20:23], v1 offset:4352\n\t"
    "ds_read_b128 a[24:27], v1 offset:4608\n\t"
    "ds_read_b128 a[28:31], v1 offset:4864\n\t"
    "ds_read_b128 a[32:35], v1 offset:8192\n\t"
    "ds_read_b128 a[36:39], v1 offset:8448\n\t"
    "ds_read_b128 a[40:43], v1 offset:8704\n\t"
    "s_waitcnt lgkmcnt(7)\n\t"
    "s_add_u32 m0, s67, 49152\n\t"
    "s_nop 0\n\t"
    "ds_read_b128 a[48:51], v1 offset:12288\n\t"
    "buffer_load_dwordx4 v4, s[48:51], s58 offen nt lds\n\t"
    "ds_read_b128 a[56:59], v1 offset:12800\n\t"
    "v_mfma_f32_16x16x32_bf16 v[224:227], a[0:3], a[128:131], 0\n\t"
    "buffer_load_dwordx4 v5, s[48:51], s58 offen offset:1024 nt lds\n\t"
    "v_mfma_f32_16x16x32_bf16 v[224:227], a[4:7], a[132:135], v[224:227]\n\t"
    "buffer_load_dwordx4 v6, s[48:51], s58 offen offset:2048 nt lds\n\t"
    "v_mfma_f32_16x16x32_bf16 v[224:227], a[8:11], a[136:139], v[224:227]\n\t"
    "buffer_load_dwordx4 v7, s[48:51], s58 offen offset:3072 nt lds\n\t"
    "v_mfma_f32_16x16x32_bf16 v[224:227], a[12:15], a[140:143], v[224:227]\n\t"
    "ds_read_b128 a[52:55], v1 offset:12544\n\t"
    "s_add_u32 s58, s58, 4096\n\t"
    "v_mfma_f32_16x16x32_bf16 v[16:19], a[234:237], a[128:131], 0\n\t"
    "ds_read_b128 a[60:63], v1 offset:13056\n\t"
    "s_add_u32 m0, s67, 114688\n\t"
    "v_mfma_f32_16x16x32_bf16 v[16:19], a[238:241], a[128:131], v[16:19]\n\t"
    "ds_read_b128 a[44:47], v1 offset:8960\n\t"
    "s_nop 0\n\t"
    "v_mfma_f32_16x16x32_bf16 v[20:23], a[232:235], a[128:131], 0\n\t"
    "buffer_load_dwordx4 v4, s[48:51], s58 offen nt lds\n\t"
    "v_mfma_f32_16x16x32_bf16 v[20:23], a[236:239], a[128:131], v[20:23]\n\t"
    "v_max3_f32 v12, v224, v225, v226\n\t"
    "v_max_f32 v12, v12, v227\n\t"
    "v_mov_b32 v13, v12\n\t"
    "s_nop 0\n\t"
    "s_nop 0\n\t"
    "v_permlane16_swap_b32 v12, v13\n\t"
    "v_mfma_f32_16x16x32_bf16 v[24:27], a[234:237], a[132:135], 0\n\t"
    "v_max_f32 v12, v12, v13\n\t"
    "v_mov_b32 v13, v12\n\t"
    "buffer_load_dwordx4 v5, s[48:51], s58 offen offset:1024 nt lds\n\t"
    "v_mfma_f32_16x16x32_bf16 v[24:27], a[238:241], a[132:135], v[24:27]\n\t"
    "v_permlane32_swap_b32 v12, v13\n\t"
    "v_max_f32 v12, v12, v13\n\t"
    "v_cvt_pk_bf16_f32 v18, v18, v19\n\t"
    "v_mfma_f32_16x16x32_bf16 v[28:31], a[232:235], a[132:135], 0\n\t"
    "v_cvt_pk_bf16_f32 v16, v16, v17\n\t"
    "v_accvgpr_write_b32 a128, v16\n\t"
    "v_accvgpr_write_b32 a129, v18\n\t"
    "v_mfma_f32_16x16x32_bf16 v[28:31], a[236:239], a[132:135], v[28:31]\n\t"
    "v_cvt_pk_bf16_f32 v20, v20, v21\n\t"
    "v_accvgpr_write_b32 a130, v20\n\t"
    "buffer_load_dwordx4 v6, s[48:51], s58 offen offset:2048 nt lds\n\t"
    "v_mfma_f32_16x16x32_bf16 v[32:35], a[234:237], a[136:139], 0\n\t"
    "v_mul_f32 v225, 0xbe0293ee, v12\n\t"
    "buffer_load_dwordx4 v7, s[48:51], s58 offen offset:3072 nt lds\n\t"
    "v_mfma_f32_16x16x32_bf16 v[32:35], a[238:241], a[136:139], v[32:35]\n\t"
    "v_mul_f32 v227, 0xbe0293ee, v12\n\t"
    "v_cvt_pk_bf16_f32 v24, v24, v25\n\t"
    "s_add_u32 s58, s58, 4096\n\t"
    "v_mfma_f32_16x16x32_bf16 v[36:39], a[232:235], a[136:139], 0\n\t"
    "v_mul_f32 v224, 0xbe0293ee, v12\n\t"
    "v_mul_f32 v226, 0xbe0293ee, v12\n\t"
    "v_accvgpr_write_b32 a132, v24\n\t"
    "v_mfma_f32_16x16x32_bf16 v[36:39], a[236:239], a[136:139], v[36:39]\n\t"
    "v_cvt_pk_bf16_f32 v22, v22, v23\n\t"
    "v_accvgpr_write_b32 a131, v22\n\t"
    "v_cvt_pk_bf16_f32 v30, v30, v31\n\t"
    "v_mfma_f32_16x16x32_bf16 v[40:43], a[234:237], a[140:143], 0\n\t"
    "v_cvt_pk_bf16_f32 v26, v26, v27\n\t"
    "v_cvt_pk_bf16_f32 v28, v28, v29\n\t"
    "v_accvgpr_write_b32 a133, v26\n\t"
    "v_mfma_f32_16x16x32_bf16 v[40:43], a[238:241], a[140:143], v[40:43]\n\t"
    "v_accvgpr_write_b32 a134, v28\n\t"
    "v_accvgpr_write_b32 a135, v30\n\t"
    "v_cvt_pk_bf16_f32 v32, v32, v33\n\t"
    "v_mfma_f32_16x16x32_bf16 v[44:47], a[232:235], a[140:143], 0\n\t"
    "v_cvt_pk_bf16_f32 v34, v34, v35\n\t"
    "v_accvgpr_write_b32 a136, v32\n\t"
    "v_accvgpr_write_b32 a137, v34\n\t"
    "v_mfma_f32_16x16x32_bf16 v[44:47], a[236:239], a[140:143], v[44:47]\n\t"
    "v_cvt_pk_bf16_f32 v36, v36, v37\n\t"
    "s_nop 6\n\t"
    "v_cvt_pk_bf16_f32 v44, v44, v45\n\t"
    "v_cvt_pk_bf16_f32 v46, v46, v47\n\t"
    "v_accvgpr_write_b32 a142, v44\n\t"
    "s_waitcnt lgkmcnt(0)\n\t"
    "v_mfma_f32_16x16x32_bf16 v[176:179], a[0:3], a[128:131], v[224:227]\n\t"
    "v_cvt_pk_bf16_f32 v38, v38, v39\n\t"
    "v_accvgpr_write_b32 a138, v36\n\t"
    "v_accvgpr_write_b32 a139, v38\n\t"
    "v_cvt_pk_bf16_f32 v40, v40, v41\n\t"
    "v_mfma_f32_16x16x32_bf16 v[176:179], a[4:7], a[132:135], v[176:179]\n\t"
    "v_cvt_pk_bf16_f32 v42, v42, v43\n\t"
    "v_accvgpr_write_b32 a140, v40\n\t"
    "v_accvgpr_write_b32 a141, v42\n\t"
    "v_accvgpr_write_b32 a143, v46\n\t"
    "v_mfma_f32_16x16x32_bf16 v[176:179], a[8:11], a[136:139], v[176:179]\n\t"
    "s_nop 0\n\t"
    "v_mfma_f32_16x16x32_bf16 v[176:179], a[12:15], a[140:143], v[176:179]\n\t"
    "v_mfma_f32_16x16x32_bf16 v[180:183], a[16:19], a[128:131], v[224:227]\n\t"
    "v_mfma_f32_16x16x32_bf16 v[180:183], a[20:23], a[132:135], v[180:183]\n\t"
    "v_mfma_f32_16x16x32_bf16 v[180:183], a[24:27], a[136:139], v[180:183]\n\t"
    "v_mfma_f32_16x16x32_bf16 v[180:183], a[28:31], a[140:143], v[180:183]\n\t"
    "s_nop 3\n\t"
    "v_exp_f32 v177, v177\n\t"
    "v_mfma_f32_16x16x32_bf16 v[184:187], a[32:35], a[128:131], v[224:227]\n\t"
    "v_exp_f32 v179, v179\n\t"
    "v_mfma_f32_16x16x32_bf16 v[184:187], a[36:39], a[132:135], v[184:187]\n\t"
    "v_exp_f32 v178, v178\n\t"
    "v_mfma_f32_16x16x32_bf16 v[184:187], a[40:43], a[136:139], v[184:187]\n\t"
    "v_exp_f32 v176, v176\n\t"
    "v_cvt_pk_bf16_f32 v209, v178, v179\n\t"
    "v_mfma_f32_16x16x32_bf16 v[184:187], a[44:47], a[140:143], v[184:187]\n\t"
    "v_exp_f32 v180, v180\n\t"
    "v_exp_f32 v182, v182\n\t"
    "v_mfma_f32_16x16x32_bf16 v[188:191], a[48:51], a[128:131], v[224:227]\n\t"
    "v_cvt_pk_bf16_f32 v208, v176, v177\n\t"
    "v_exp_f32 v183, v183\n\t"
    "v_mfma_f32_16x16x32_bf16 v[188:191], a[52:55], a[132:135], v[188:191]\n\t"
    "v_exp_f32 v181, v181\n\t"
    "v_mfma_f32_16x16x32_bf16 v[188:191], a[56:59], a[136:139], v[188:191]\n\t"
    "v_cvt_pk_bf16_f32 v210, v180, v181\n\t"
    "v_cvt_pk_bf16_f32 v211, v182, v183\n\t"
    "v_mfma_f32_16x16x32_bf16 v[188:191], a[60:63], a[140:143], v[188:191]\n\t"
    "s_waitcnt vmcnt(8)\n\t"
    "s_barrier\n\t"
    "ds_read_b128 a[144:147], v9 offset:4096\n\t"
    "ds_read_b128 a[148:151], v9 offset:4352\n\t"
    "ds_read_b128 a[152:155], v9 offset:4608\n\t"
    "ds_read_b128 a[156:159], v9 offset:4864\n\t"
    "ds_read_b64_tr_b16 a[64:65], v2 offset:0\n\t"
    "ds_read_b64_tr_b16 a[66:67], v2 offset:4096\n\t"
    "ds_read_b64_tr_b16 a[68:69], v2 offset:8192\n\t"
    "ds_read_b64_tr_b16 a[70:71], v2 offset:12288\n\t"
    "ds_read_b64_tr_b16 a[72:73], v3 offset:0\n\t"
    "ds_read_b64_tr_b16 a[74:75], v3 offset:4096\n\t"
    "ds_read_b64_tr_b16 a[76:77], v3 offset:8192\n\t"
    "ds_read_b64_tr_b16 a[78:79], v3 offset:12288\n\t"
    "ds_read_b64_tr_b16 a[80:81], v2 offset:256\n\t"
    "ds_read_b64_tr_b16 a[82:83], v2 offset:4352\n\t"
    "ds_read_b64_tr_b16 a[84:85], v2 offset:8448\n\t"
    "s_waitcnt lgkmcnt(11)\n\t"
    "v_mfma_f32_16x16x32_bf16 v[228:231], a[0:3], a[144:147], 0\n\t"
    "s_add_u32 m0, s67, 16384\n\t"
    "ds_read_b64_tr_b16 a[100:101], v2 offset:8704\n\t"
    "ds_read_b64_tr_b16 a[124:125], v3 offset:8960\n\t"
    "v_mfma_f32_16x16x32_bf16 v[228:231], a[4:7], a[148:151], v[228:231]\n\t"
    "v_exp_f32 v191, v191\n\t"
    "s_add_u32 s59, s66, 16384\n\t"
    "ds_read_b64_tr_b16 a[98:99], v2 offset:4608\n\t"
    "v_mfma_f32_16x16x32_bf16 v[228:231], a[8:11], a[152:155], v[228:231]\n\t"
    "buffer_load_dwordx4 v4, s[40:43], s59 offen nt lds\n\t"
    "ds_read_b64_tr_b16 a[102:103], v2 offset:12800\n\t"
    "v_mfma_f32_16x16x32_bf16 v[228:231], a[12:15], a[156:159], v[228:231]\n\t"
    "buffer_load_dwordx4 v5, s[40:43], s59 offen offset:1024 nt lds\n\t"
    "buffer_load_dwordx4 v6, s[40:43], s59 offen offset:2048 nt lds\n\t"
    "v_mfma_f32_16x16x32_bf16 v[48:51], a[234:237], a[144:147], 0\n\t"
    "buffer_load_dwordx4 v7, s[40:43], s59 offen offset:3072 nt lds\n\t"
    "ds_read_b64_tr_b16 a[126:127], v3 offset:13056\n\t"
    "v_mfma_f32_16x16x32_bf16 v[48:51], a[238:241], a[144:147], v[48:51]\n\t"
    "ds_read_b64_tr_b16 a[94:95], v3 offset:12544\n\t"
    "s_add_u32 m0, s67, 81920\n\t"
    "ds_read_b64_tr_b16 a[116:117], v2 offset:8960\n\t"
    "v_mfma_f32_16x16x32_bf16 v[52:55], a[232:235], a[144:147], 0\n\t"
    "ds_read_b64_tr_b16 a[92:93], v3 offset:8448\n\t"
    "ds_read_b64_tr_b16 a[108:109], v3 offset:8704\n\t"
    "s_add_u32 s59, s66, 16384\n\t"
    "v_mfma_f32_16x16x32_bf16 v[52:55], a[236:239], a[144:147], v[52:55]\n\t"
    "v_max3_f32 v12, v228, v229, v230\n\t"
    "v_max_f32 v12, v12, v231\n\t"
    "v_mov_b32 v13, v12\n\t"
    "s_nop 0\n\t"
    "s_nop 0\n\t"
    "v_permlane16_swap_b32 v12, v13\n\t"
    "v_mfma_f32_16x16x32_bf16 v[56:59], a[234:237], a[148:151], 0\n\t"
    "v_max_f32 v12, v12, v13\n\t"
    "v_mov_b32 v13, v12\n\t"
    "s_nop 0\n\t"
    "s_nop 0\n\t"
    "v_permlane32_swap_b32 v12, v13\n\t"
    "v_mfma_f32_16x16x32_bf16 v[56:59], a[238:241], a[148:151], v[56:59]\n\t"
    "buffer_load_dwordx4 v4, s[44:47], s59 offen nt lds\n\t"
    "buffer_load_dwordx4 v5, s[44:47], s59 offen offset:1024 nt lds\n\t"
    "v_mfma_f32_16x16x32_bf16 v[60:63], a[232:235], a[148:151], 0\n\t"
    "v_max_f32 v12, v12, v13\n\t"
    "v_cvt_pk_bf16_f32 v48, v48, v49\n\t"
    "v_accvgpr_write_b32 a144, v48\n\t"
    "v_mfma_f32_16x16x32_bf16 v[60:63], a[236:239], a[148:151], v[60:63]\n\t"
    "v_mul_f32 v228, 0xbe0293ee, v12\n\t"
    "v_mul_f32 v230, 0xbe0293ee, v12\n\t"
    "v_cvt_pk_bf16_f32 v52, v52, v53\n\t"
    "v_mfma_f32_16x16x32_bf16 v[64:67], a[234:237], a[152:155], 0\n\t"
    "v_cvt_pk_bf16_f32 v54, v54, v55\n\t"
    "v_accvgpr_write_b32 a146, v52\n\t"
    "buffer_load_dwordx4 v4, s[44:47], s59 offen offset:2048 nt lds\n\t"
    "v_mfma_f32_16x16x32_bf16 v[64:67], a[238:241], a[152:155], v[64:67]\n\t"
    "v_cvt_pk_bf16_f32 v50, v50, v51\n\t"
    "v_cvt_pk_bf16_f32 v58, v58, v59\n\t"
    "v_accvgpr_write_b32 a149, v58\n\t"
    "v_mfma_f32_16x16x32_bf16 v[68:71], a[232:235], a[152:155], 0\n\t"
    "v_accvgpr_write_b32 a147, v54\n\t"
    "v_cvt_pk_bf16_f32 v56, v56, v57\n\t"
    "buffer_load_dwordx4 v5, s[44:47], s59 offen offset:3072 nt lds\n\t"
    "v_mfma_f32_16x16x32_bf16 v[68:71], a[236:239], a[152:155], v[68:71]\n\t"
    "v_mul_f32 v231, 0xbe0293ee, v12\n\t"
    "v_accvgpr_write_b32 a145, v50\n\t"
    "v_cvt_pk_bf16_f32 v60, v60, v61\n\t"
    "v_mfma_f32_16x16x32_bf16 v[72:75], a[234:237], a[156:159], 0\n\t"
    "v_mul_f32 v229, 0xbe0293ee, v12\n\t"
    "v_cvt_pk_bf16_f32 v62, v62, v63\n\t"
    "v_accvgpr_write_b32 a150, v60\n\t"
    "v_mfma_f32_16x16x32_bf16 v[72:75], a[238:241], a[156:159], v[72:75]\n\t"
    "v_accvgpr_write_b32 a148, v56\n\t"
    "v_cvt_pk_bf16_f32 v66, v66, v67\n\t"
    "v_accvgpr_write_b32 a153, v66\n\t"
    "v_mfma_f32_16x16x32_bf16 v[76:79], a[232:235], a[156:159], 0\n\t"
    "v_accvgpr_write_b32 a151, v62\n\t"
    "v_cvt_pk_bf16_f32 v64, v64, v65\n\t"
    "v_accvgpr_write_b32 a152, v64\n\t"
    "v_mfma_f32_16x16x32_bf16 v[76:79], a[236:239], a[156:159], v[76:79]\n\t"
    "v_cvt_pk_bf16_f32 v68, v68, v69\n\t"
    "v_cvt_pk_bf16_f32 v72, v72, v73\n\t"
    "v_cvt_pk_bf16_f32 v74, v74, v75\n\t"
    "s_nop 4\n\t"
    "v_cvt_pk_bf16_f32 v78, v78, v79\n\t"
    "v_mfma_f32_16x16x32_bf16 v[192:195], a[0:3], a[144:147], v[228:231]\n\t"
    "v_cvt_pk_bf16_f32 v70, v70, v71\n\t"
    "v_accvgpr_write_b32 a154, v68\n\t"
    "v_accvgpr_write_b32 a155, v70\n\t"
    "v_accvgpr_write_b32 a156, v72\n\t"
    "v_mfma_f32_16x16x32_bf16 v[192:195], a[4:7], a[148:151], v[192:195]\n\t"
    "v_cvt_pk_bf16_f32 v76, v76, v77\n\t"
    "v_accvgpr_write_b32 a157, v74\n\t"
    "v_accvgpr_write_b32 a158, v76\n\t"
    "v_accvgpr_write_b32 a159, v78\n\t"
    "v_mfma_f32_16x16x32_bf16 v[192:195], a[8:11], a[152:155], v[192:195]\n\t"
    "s_waitcnt vmcnt(12)\n\t"
    "ds_read_b128 a[160:163], v8 offset:0\n\t"
    "ds_read_b128 a[164:167], v8 offset:256\n\t"
    "v_mfma_f32_16x16x32_bf16 v[192:195], a[12:15], a[156:159], v[192:195]\n\t"
    "v_exp_f32 v186, v186\n\t"
    "ds_read_b128 a[172:175], v8 offset:768\n\t"
    "v_mfma_f32_16x16x32_bf16 v[196:199], a[16:19], a[144:147], v[228:231]\n\t"
    "ds_read_b128 a[168:171], v8 offset:512\n\t"
    "s_add_u32 m0, s67, 32768\n\t"
    "v_mfma_f32_16x16x32_bf16 v[196:199], a[20:23], a[148:151], v[196:199]\n\t"
    "ds_read_b64_tr_b16 a[104:105], v3 offset:512\n\t"
    "ds_read_b64_tr_b16 a[114:115], v2 offset:4864\n\t"
    "v_mfma_f32_16x16x32_bf16 v[196:199], a[24:27], a[152:155], v[196:199]\n\t"
    "v_exp_f32 v185, v185\n\t"
    "v_add_u32 v8, 0x10000, v8\n\t"
    "v_mfma_f32_16x16x32_bf16 v[196:199], a[28:31], a[156:159], v[196:199]\n\t"
    "ds_read_b64_tr_b16 a[86:87], v2 offset:12544\n\t"
    "ds_read_b64_tr_b16 a[112:113], v2 offset:768\n\t"
    "v_mfma_f32_16x16x32_bf16 v[200:203], a[32:35], a[144:147], v[228:231]\n\t"
    "ds_read_b64_tr_b16 a[118:119], v2 offset:13056\n\t"
    "s_waitcnt lgkmcnt(5)\n\t"
    "v_mfma_f32_16x16x32_bf16 v[232:235], a[0:3], a[160:163], 0\n\t"
    "v_exp_f32 v192, v192\n\t"
    "v_mfma_f32_16x16x32_bf16 v[232:235], a[4:7], a[164:167], v[232:235]\n\t"
    "ds_read_b64_tr_b16 a[88:89], v3 offset:256\n\t"
    "s_add_u32 s59, s66, 32768\n\t"
    "v_mfma_f32_16x16x32_bf16 v[232:235], a[8:11], a[168:171], v[232:235]\n\t"
    "v_exp_f32 v190, v190\n\t"
    "s_nop 0\n\t"
    "v_cvt_pk_bf16_f32 v215, v190, v191\n\t"
    "v_mfma_f32_16x16x32_bf16 v[232:235], a[12:15], a[172:175], v[232:235]\n\t"
    "v_exp_f32 v187, v187\n\t"
    "v_mfma_f32_16x16x32_bf16 v[80:83], a[234:237], a[160:163], 0\n\t"
    "ds_read_b64_tr_b16 a[106:107], v3 offset:4608\n\t"
    "ds_read_b64_tr_b16 a[110:111], v3 offset:12800\n\t"
    "v_mfma_f32_16x16x32_bf16 v[80:83], a[238:241], a[160:163], v[80:83]\n\t"
    "ds_read_b64_tr_b16 a[120:121], v3 offset:768\n\t"
    "ds_read_b64_tr_b16 a[122:123], v3 offset:4864\n\t"
    "v_mfma_f32_16x16x32_bf16 v[200:203], a[36:39], a[148:151], v[200:203]\n\t"
    "v_exp_f32 v184, v184\n\t"
    "v_mfma_f32_16x16x32_bf16 v[84:87], a[232:235], a[160:163], 0\n\t"
    "buffer_load_dwordx4 v4, s[40:43], s59 offen nt lds\n\t"
    "v_mfma_f32_16x16x32_bf16 v[84:87], a[236:239], a[160:163], v[84:87]\n\t"
    "v_cvt_pk_bf16_f32 v212, v184, v185\n\t"
    "v_max3_f32 v12, v232, v233, v234\n\t"
    "v_mfma_f32_16x16x32_bf16 v[88:91], a[234:237], a[164:167], 0\n\t"
    "v_max_f32 v12, v12, v235\n\t"
    "v_cvt_pk_bf16_f32 v82, v82, v83\n\t"
    "v_mfma_f32_16x16x32_bf16 v[88:91], a[238:241], a[164:167], v[88:91]\n\t"
    "v_exp_f32 v189, v189\n\t"
    "v_mfma_f32_16x16x32_bf16 v[92:95], a[232:235], a[164:167], 0\n\t"
    "ds_read_b64_tr_b16 a[90:91], v3 offset:4352\n\t"
    "ds_read_b64_tr_b16 a[96:97], v2 offset:512\n\t"
    "v_mfma_f32_16x16x32_bf16 v[92:95], a[236:239], a[164:167], v[92:95]\n\t"
    "buffer_load_dwordx4 v5, s[40:43], s59 offen offset:1024 nt lds\n\t"
    "v_mfma_f32_16x16x32_bf16 v[200:203], a[40:43], a[152:155], v[200:203]\n\t"
    "buffer_load_dwordx4 v6, s[40:43], s59 offen offset:2048 nt lds\n\t"
    "v_mfma_f32_16x16x32_bf16 v[200:203], a[44:47], a[156:159], v[200:203]\n\t"
    "v_mov_b32 v13, v12\n\t"
    "s_nop 1\n\t"
    "v_permlane16_swap_b32 v12, v13\n\t"
    "v_mfma_f32_16x16x32_bf16 v[204:207], a[48:51], a[144:147], v[228:231]\n\t"
    "v_exp_f32 v188, v188\n\t"
    "v_mfma_f32_16x16x32_bf16 v[96:99], a[234:237], a[168:171], 0\n\t"
    "v_accvgpr_write_b32 a161, v82\n\t"
    "v_cvt_pk_bf16_f32 v92, v92, v93\n\t"
    "v_mfma_f32_16x16x32_bf16 v[96:99], a[238:241], a[168:171], v[96:99]\n\t"
    "v_cvt_pk_bf16_f32 v86, v86, v87\n\t"
    "v_cvt_pk_bf16_f32 v90, v90, v91\n\t"
    "v_mfma_f32_16x16x32_bf16 v[204:207], a[52:55], a[148:151], v[204:207]\n\t"
    "v_cvt_pk_bf16_f32 v84, v84, v85\n\t"
    "v_cvt_pk_bf16_f32 v88, v88, v89\n\t"
    "v_mfma_f32_16x16x32_bf16 v[100:103], a[232:235], a[168:171], 0\n\t"
    "v_max_f32 v12, v12, v13\n\t"
    "v_accvgpr_write_b32 a165, v90\n\t"
    "v_mfma_f32_16x16x32_bf16 v[204:207], a[56:59], a[152:155], v[204:207]\n\t"
    "v_cvt_pk_bf16_f32 v214, v188, v189\n\t"
    "s_waitcnt lgkmcnt(0)\n\t"
    "v_mfma_f32_16x16x32_bf16 v[100:103], a[236:239], a[168:171], v[100:103]\n\t"
    "v_mov_b32 v13, v12\n\t"
    "v_accvgpr_write_b32 a162, v84\n\t"
    "v_mfma_f32_16x16x32_bf16 v[104:107], a[234:237], a[172:175], 0\n\t"
    "v_cvt_pk_bf16_f32 v98, v98, v99\n\t"
    "v_accvgpr_write_b32 a169, v98\n\t"
    "v_mfma_f32_16x16x32_bf16 v[204:207], a[60:63], a[156:159], v[204:207]\n\t"
    "v_exp_f32 v198, v198\n\t"
    "v_mfma_f32_16x16x32_bf16 v[104:107], a[238:241], a[172:175], v[104:107]\n\t"
    "buffer_load_dwordx4 v7, s[40:43], s59 offen offset:3072 nt lds\n\t"
    "v_mfma_f32_16x16x32_bf16 v[108:111], a[232:235], a[172:175], 0\n\t"
    "v_cvt_pk_bf16_f32 v213, v186, v187\n\t"
    "v_cvt_pk_bf16_f32 v94, v94, v95\n\t"
    "v_mfma_f32_16x16x32_bf16 v[16:19], v[208:211], a[64:67], 0\n\t"
    "v_cvt_pk_bf16_f32 v96, v96, v97\n\t"
    "v_cvt_pk_bf16_f32 v102, v102, v103\n\t"
    "v_mfma_f32_16x16x32_bf16 v[108:111], a[236:239], a[172:175], v[108:111]\n\t"
    "v_exp_f32 v195, v195\n\t"
    "v_mfma_f32_16x16x32_bf16 v[16:19], v[212:215], a[68:71], v[16:19]\n\t"
    "v_permlane32_swap_b32 v12, v13\n\t"
    "v_accvgpr_write_b32 a164, v88\n\t"
    "v_mfma_f32_16x16x32_bf16 v[20:23], v[208:211], a[72:75], 0\n\t"
    "v_max_f32 v12, v12, v13\n\t"
    "v_cvt_pk_bf16_f32 v80, v80, v81\n\t"
    "v_mfma_f32_16x16x32_bf16 v[20:23], v[212:215], a[76:79], v[20:23]\n\t"
    "v_accvgpr_write_b32 a166, v92\n\t"
    "v_cvt_pk_bf16_f32 v104, v104, v105\n\t"
    "v_mfma_f32_16x16x32_bf16 v[24:27], v[208:211], a[80:83], 0\n\t"
    "v_mul_f32 v232, 0xbe0293ee, v12\n\t"
    "v_cvt_pk_bf16_f32 v106, v106, v107\n\t"
    "v_mfma_f32_16x16x32_bf16 v[24:27], v[212:215], a[84:87], v[24:27]\n\t"
    "v_mul_f32 v233, 0xbe0293ee, v12\n\t"
    "v_cvt_pk_bf16_f32 v110, v110, v111\n\t"
    "v_mfma_f32_16x16x32_bf16 v[28:31], v[208:211], a[88:91], 0\n\t"
    "v_accvgpr_write_b32 a160, v80\n\t"
    "v_accvgpr_write_b32 a173, v106\n\t"
    "v_mfma_f32_16x16x32_bf16 v[28:31], v[212:215], a[92:95], v[28:31]\n\t"
    "v_accvgpr_write_b32 a163, v86\n\t"
    "v_cvt_pk_bf16_f32 v100, v100, v101\n\t"
    "v_mfma_f32_16x16x32_bf16 v[32:35], v[208:211], a[96:99], 0\n\t"
    "v_accvgpr_write_b32 a172, v104\n\t"
    "v_cvt_pk_bf16_f32 v108, v108, v109\n\t"
    "v_mfma_f32_16x16x32_bf16 v[32:35], v[212:215], a[100:103], v[32:35]\n\t"
    "v_mul_f32 v234, 0xbe0293ee, v12\n\t"
    "v_accvgpr_write_b32 a170, v100\n\t"
    "v_mfma_f32_16x16x32_bf16 v[36:39], v[208:211], a[104:107], 0\n\t"
    "v_mul_f32 v235, 0xbe0293ee, v12\n\t"
    "v_accvgpr_write_b32 a168, v96\n\t"
    "v_mfma_f32_16x16x32_bf16 v[36:39], v[212:215], a[108:111], v[36:39]\n\t"
    "v_exp_f32 v197, v197\n\t"
    "v_mfma_f32_16x16x32_bf16 v[40:43], v[208:211], a[112:115], 0\n\t"
    "v_exp_f32 v199, v199\n\t"
    "v_mfma_f32_16x16x32_bf16 v[40:43], v[212:215], a[116:119], v[40:43]\n\t"
    "v_accvgpr_write_b32 a167, v94\n\t"
    "v_accvgpr_write_b32 a174, v108\n\t"
    "v_mfma_f32_16x16x32_bf16 v[44:47], v[208:211], a[120:123], 0\n\t"
    "v_exp_f32 v202, v202\n\t"
    "v_mfma_f32_16x16x32_bf16 v[44:47], v[212:215], a[124:127], v[44:47]\n\t"
    "v_accvgpr_write_b32 a171, v102\n\t"
    "v_accvgpr_write_b32 a175, v110\n\t"
    "v_smfmac_f32_16x16x64_bf16 a[212:215], a[208:211], v[208:215], v0\n\t"
    "v_cvt_pk_bf16_f32 v219, v198, v199\n\t"
    "s_barrier\n\t"
    "v_mfma_f32_16x16x32_bf16 v[176:179], a[0:3], a[160:163], v[232:235]\n\t"
    "v_exp_f32 v204, v204\n\t"
    "v_mfma_f32_16x16x32_bf16 v[176:179], a[4:7], a[164:167], v[176:179]\n\t"
    "v_exp_f32 v194, v194\n\t"
    "v_mfma_f32_16x16x32_bf16 v[176:179], a[8:11], a[168:171], v[176:179]\n\t"
    "s_waitcnt vmcnt(12)\n\t"
    "ds_read_b128 a[180:183], v8 offset:256\n\t"
    "v_mfma_f32_16x16x32_bf16 v[176:179], a[12:15], a[172:175], v[176:179]\n\t"
    "ds_read_b128 a[176:179], v8 offset:0\n\t"
    "s_add_u32 m0, s67, 98304\n\t"
    "v_mfma_f32_16x16x32_bf16 v[180:183], a[16:19], a[160:163], v[232:235]\n\t"
    "ds_read_b128 a[184:187], v8 offset:512\n\t"
    "ds_read_b128 a[188:191], v8 offset:768\n\t"
    "v_mfma_f32_16x16x32_bf16 v[180:183], a[20:23], a[164:167], v[180:183]\n\t"
    "v_exp_f32 v206, v206\n\t"
    "v_mfma_f32_16x16x32_bf16 v[180:183], a[24:27], a[168:171], v[180:183]\n\t"
    "v_exp_f32 v207, v207\n\t"
    "v_mfma_f32_16x16x32_bf16 v[180:183], a[28:31], a[172:175], v[180:183]\n\t"
    "v_exp_f32 v205, v205\n\t"
    "v_mfma_f32_16x16x32_bf16 v[184:187], a[32:35], a[160:163], v[232:235]\n\t"
    "s_waitcnt lgkmcnt(0)\n\t"
    "v_cvt_pk_bf16_f32 v223, v206, v207\n\t"
    "v_mfma_f32_16x16x32_bf16 v[236:239], a[0:3], a[176:179], 0\n\t"
    "v_exp_f32 v196, v196\n\t"
    "v_mfma_f32_16x16x32_bf16 v[236:239], a[4:7], a[180:183], v[236:239]\n\t"
    "s_add_u32 s59, s66, 32768\n\t"
    "v_cvt_pk_bf16_f32 v217, v194, v195\n\t"
    "v_mfma_f32_16x16x32_bf16 v[236:239], a[8:11], a[184:187], v[236:239]\n\t"
    "buffer_load_dwordx4 v4, s[44:47], s59 offen nt lds\n\t"
    "v_mfma_f32_16x16x32_bf16 v[184:187], a[36:39], a[164:167], v[184:187]\n\t"
    "v_exp_f32 v200, v200\n\t"
    "v_mfma_f32_16x16x32_bf16 v[236:239], a[12:15], a[188:191], v[236:239]\n\t"
    "v_exp_f32 v201, v201\n\t"
    "v_mfma_f32_16x16x32_bf16 v[80:83], a[234:237], a[176:179], 0\n\t"
    "v_exp_f32 v203, v203\n\t"
    "v_mfma_f32_16x16x32_bf16 v[80:83], a[238:241], a[176:179], v[80:83]\n\t"
    "v_exp_f32 v193, v193\n\t"
    "v_mfma_f32_16x16x32_bf16 v[184:187], a[40:43], a[168:171], v[184:187]\n\t"
    "v_cvt_pk_bf16_f32 v221, v202, v203\n\t"
    "v_cvt_pk_bf16_f32 v222, v204, v205\n\t"
    "v_mfma_f32_16x16x32_bf16 v[84:87], a[232:235], a[176:179], 0\n\t"
    "v_exp_f32 v178, v178\n\t"
    "v_mfma_f32_16x16x32_bf16 v[84:87], a[236:239], a[176:179], v[84:87]\n\t"
    "v_cvt_pk_bf16_f32 v220, v200, v201\n\t"
    "v_max3_f32 v12, v236, v237, v238\n\t"
    "v_mfma_f32_16x16x32_bf16 v[88:91], a[234:237], a[180:183], 0\n\t"
    "buffer_load_dwordx4 v5, s[44:47], s59 offen offset:1024 nt lds\n\t"
    "v_mfma_f32_16x16x32_bf16 v[184:187], a[44:47], a[172:175], v[184:187]\n\t"
    "v_cvt_pk_bf16_f32 v82, v82, v83\n\t"
    "v_accvgpr_write_b32 a177, v82\n\t"
    "v_mfma_f32_16x16x32_bf16 v[88:91], a[238:241], a[180:183], v[88:91]\n\t"
    "v_exp_f32 v182, v182\n\t"
    "v_mfma_f32_16x16x32_bf16 v[92:95], a[232:235], a[180:183], 0\n\t"
    "v_cvt_pk_bf16_f32 v216, v192, v193\n\t"
    "v_cvt_pk_bf16_f32 v80, v80, v81\n\t"
    "v_mfma_f32_16x16x32_bf16 v[92:95], a[236:239], a[180:183], v[92:95]\n\t"
    "v_exp_f32 v179, v179\n\t"
    "v_mfma_f32_16x16x32_bf16 v[188:191], a[48:51], a[160:163], v[232:235]\n\t"
    "v_cvt_pk_bf16_f32 v218, v196, v197\n\t"
    "v_cvt_pk_bf16_f32 v209, v178, v179\n\t"
    "v_mfma_f32_16x16x32_bf16 v[188:191], a[52:55], a[164:167], v[188:191]\n\t"
    "v_exp_f32 v177, v177\n\t"
    "v_mfma_f32_16x16x32_bf16 v[188:191], a[56:59], a[168:171], v[188:191]\n\t"
    "v_max_f32 v12, v12, v239\n\t"
    "v_mov_b32 v13, v12\n\t"
    "v_mfma_f32_16x16x32_bf16 v[188:191], a[60:63], a[172:175], v[188:191]\n\t"
    "buffer_load_dwordx4 v4, s[44:47], s59 offen offset:2048 nt lds\n\t"
    "v_mfma_f32_16x16x32_bf16 v[48:51], v[216:219], a[64:67], 0\n\t"
    "v_exp_f32 v183, v183\n\t"
    "v_mfma_f32_16x16x32_bf16 v[48:51], v[220:223], a[68:71], v[48:51]\n\t"
    "ds_read_b128 v[244:247], v14 offset:1024\n\t"
    "ds_read_b128 v[240:243], v14 offset:0\n\t"
    "v_mfma_f32_16x16x32_bf16 v[96:99], a[234:237], a[184:187], 0\n\t"
    "v_exp_f32 v176, v176\n\t"
    "v_mfma_f32_16x16x32_bf16 v[52:55], v[216:219], a[72:75], 0\n\t"
    "v_permlane16_swap_b32 v12, v13\n\t"
    "v_max_f32 v12, v12, v13\n\t"
    "v_mfma_f32_16x16x32_bf16 v[96:99], a[238:241], a[184:187], v[96:99]\n\t"
    "buffer_load_dwordx4 v5, s[44:47], s59 offen offset:3072 nt lds\n\t"
    "v_mfma_f32_16x16x32_bf16 v[52:55], v[220:223], a[76:79], v[52:55]\n\t"
    "ds_read_b128 v[252:255], v14 offset:9216\n\t"
    "ds_read_b128 v[248:251], v14 offset:8192\n\t"
    "v_mfma_f32_16x16x32_bf16 v[100:103], a[232:235], a[184:187], 0\n\t"
    "v_accvgpr_write_b32 a176, v80\n\t"
    "v_cvt_pk_bf16_f32 v84, v84, v85\n\t"
    "v_mfma_f32_16x16x32_bf16 v[56:59], v[216:219], a[80:83], 0\n\t"
    "v_mov_b32 v13, v12\n\t"
    "s_nop 1\n\t"
    "v_permlane32_swap_b32 v12, v13\n\t"
    "v_mfma_f32_16x16x32_bf16 v[100:103], a[236:239], a[184:187], v[100:103]\n\t"
    "v_exp_f32 v190, v190\n\t"
    "v_mfma_f32_16x16x32_bf16 v[56:59], v[220:223], a[84:87], v[56:59]\n\t"
    "v_cvt_pk_bf16_f32 v90, v90, v91\n\t"
    "v_accvgpr_write_b32 a181, v90\n\t"
    "v_mfma_f32_16x16x32_bf16 v[104:107], a[234:237], a[188:191], 0\n\t"
    "v_max_f32 v12, v12, v13\n\t"
    "v_mul_f32 v236, 0xbe0293ee, v12\n\t"
    "v_mfma_f32_16x16x32_bf16 v[60:63], v[216:219], a[88:91], 0\n\t"
    "v_exp_f32 v180, v180\n\t"
    "v_mfma_f32_16x16x32_bf16 v[104:107], a[238:241], a[188:191], v[104:107]\n\t"
    "v_exp_f32 v188, v188\n\t"
    "v_mfma_f32_16x16x32_bf16 v[60:63], v[220:223], a[92:95], v[60:63]\n\t"
    "v_cvt_pk_bf16_f32 v94, v94, v95\n\t"
    "v_cvt_pk_bf16_f32 v98, v98, v99\n\t"
    "v_mfma_f32_16x16x32_bf16 v[108:111], a[232:235], a[188:191], 0\n\t"
    "v_cvt_pk_bf16_f32 v86, v86, v87\n\t"
    "v_cvt_pk_bf16_f32 v88, v88, v89\n\t"
    "v_mfma_f32_16x16x32_bf16 v[64:67], v[216:219], a[96:99], 0\n\t"
    "v_accvgpr_write_b32 a178, v84\n\t"
    "v_cvt_pk_bf16_f32 v92, v92, v93\n\t"
    "v_mfma_f32_16x16x32_bf16 v[108:111], a[236:239], a[188:191], v[108:111]\n\t"
    "v_cvt_pk_bf16_f32 v104, v104, v105\n\t"
    "v_cvt_pk_bf16_f32 v208, v176, v177\n\t"
    "v_mfma_f32_16x16x32_bf16 v[64:67], v[220:223], a[100:103], v[64:67]\n\t"
    "v_exp_f32 v187, v187\n\t"
    "v_mfma_f32_16x16x32_bf16 v[68:71], v[216:219], a[104:107], 0\n\t"
    "v_cvt_pk_bf16_f32 v96, v96, v97\n\t"
    "v_cvt_pk_bf16_f32 v100, v100, v101\n\t"
    "v_mfma_f32_16x16x32_bf16 v[68:71], v[220:223], a[108:111], v[68:71]\n\t"
    "v_exp_f32 v186, v186\n\t"
    "v_mfma_f32_16x16x32_bf16 v[72:75], v[216:219], a[112:115], 0\n\t"
    "v_accvgpr_write_b32 a180, v88\n\t"
    "v_cvt_pk_bf16_f32 v106, v106, v107\n\t"
    "v_mfma_f32_16x16x32_bf16 v[72:75], v[220:223], a[116:119], v[72:75]\n\t"
    "v_exp_f32 v189, v189\n\t"
    "v_mfma_f32_16x16x32_bf16 v[76:79], v[216:219], a[120:123], 0\n\t"
    "v_exp_f32 v191, v191\n\t"
    "v_mfma_f32_16x16x32_bf16 v[76:79], v[220:223], a[124:127], v[76:79]\n\t"
    "v_accvgpr_write_b32 a182, v92\n\t"
    "v_cvt_pk_bf16_f32 v110, v110, v111\n\t"
    "v_smfmac_f32_16x16x64_bf16 a[216:219], a[208:211], v[216:223], v0\n\t"
    "v_cvt_pk_bf16_f32 v108, v108, v109\n\t"
    "s_waitcnt lgkmcnt(2)\n\t"
    "v_mfma_f32_16x16x32_bf16 v[144:147], v[240:243], a[96:99], 0\n\t"
    "v_accvgpr_write_b32 a179, v86\n\t"
    "v_accvgpr_write_b32 a188, v104\n\t"
    "v_mfma_f32_16x16x32_bf16 v[144:147], v[244:247], a[100:103], v[144:147]\n\t"
    "v_exp_f32 v181, v181\n\t"
    "v_mfma_f32_16x16x32_bf16 v[148:151], v[240:243], a[104:107], 0\n\t"
    "v_exp_f32 v184, v184\n\t"
    "v_mfma_f32_16x16x32_bf16 v[148:151], v[244:247], a[108:111], v[148:151]\n\t"
    "v_accvgpr_write_b32 a183, v94\n\t"
    "v_accvgpr_write_b32 a184, v96\n\t"
    "v_mfma_f32_16x16x32_bf16 v[152:155], v[240:243], a[112:115], 0\n\t"
    "v_mul_f32 v237, 0xbe0293ee, v12\n\t"
    "v_cvt_pk_bf16_f32 v211, v182, v183\n\t"
    "v_mfma_f32_16x16x32_bf16 v[152:155], v[244:247], a[116:119], v[152:155]\n\t"
    "v_accvgpr_write_b32 a191, v110\n\t"
    "v_cvt_pk_bf16_f32 v215, v190, v191\n\t"
    "v_mfma_f32_16x16x32_bf16 v[156:159], v[240:243], a[120:123], 0\n\t"
    "v_cvt_pk_bf16_f32 v102, v102, v103\n\t"
    "v_cvt_pk_bf16_f32 v213, v186, v187\n\t"
    "v_mfma_f32_16x16x32_bf16 v[156:159], v[244:247], a[124:127], v[156:159]\n\t"
    "v_accvgpr_write_b32 a187, v102\n\t"
    "s_waitcnt lgkmcnt(0)\n\t"
    "v_mfma_f32_16x16x32_bf16 v[160:163], v[248:251], a[96:99], 0\n\t"
    "v_mul_f32 v238, 0xbe0293ee, v12\n\t"
    "v_cvt_pk_bf16_f32 v210, v180, v181\n\t"
    "v_mfma_f32_16x16x32_bf16 v[160:163], v[252:255], a[100:103], v[160:163]\n\t"
    "v_mul_f32 v239, 0xbe0293ee, v12\n\t"
    "v_accvgpr_write_b32 a189, v106\n\t"
    "v_mfma_f32_16x16x32_bf16 v[164:167], v[248:251], a[104:107], 0\n\t"
    "v_accvgpr_write_b32 a185, v98\n\t"
    "v_accvgpr_write_b32 a186, v100\n\t"
    "v_mfma_f32_16x16x32_bf16 v[164:167], v[252:255], a[108:111], v[164:167]\n\t"
    "s_waitcnt vmcnt(8)\n\t"
    "s_barrier\n\t"
    "v_mfma_f32_16x16x32_bf16 v[192:195], a[0:3], a[176:179], v[236:239]\n\t"
    "v_accvgpr_write_b32 a190, v108\n\t"
    "s_mov_b32 s56, %[lds]\n\t"
    "v_mfma_f32_16x16x32_bf16 v[192:195], a[4:7], a[180:183], v[192:195]\n\t"
    "s_xor_b32 s56, s56, 0x8000\n\t"
    "s_mov_b32 s57, 65536\n\t"
    "v_mfma_f32_16x16x32_bf16 v[192:195], a[8:11], a[184:187], v[192:195]\n\t"
    "s_mov_b32 s63, 69632\n\t"
    "s_mov_b32 s64, 73728\n\t"
    "v_mfma_f32_16x16x32_bf16 v[192:195], a[12:15], a[188:191], v[192:195]\n\t"
    "s_mov_b32 s65, 77824\n\t"
    "ds_read_b128 v[244:247], v14 offset:17408\n\t"
    "v_mfma_f32_16x16x32_bf16 v[196:199], a[16:19], a[176:179], v[236:239]\n\t"
    "v_exp_f32 v185, v185\n\t"
    "v_mfma_f32_16x16x32_bf16 v[196:199], a[20:23], a[180:183], v[196:199]\n\t"
    "s_add_u32 m0, s67, 49152\n\t"
    "s_add_u32 s59, s66, 49152\n\t"
    "v_mfma_f32_16x16x32_bf16 v[196:199], a[24:27], a[184:187], v[196:199]\n\t"
    "v_cvt_pk_bf16_f32 v214, v188, v189\n\t"
    "v_cvt_pk_bf16_f32 v212, v184, v185\n\t"
    "v_mfma_f32_16x16x32_bf16 v[196:199], a[28:31], a[188:191], v[196:199]\n\t"
    "buffer_load_dwordx4 v4, s[40:43], s59 offen nt lds\n\t"
    "v_mfma_f32_16x16x32_bf16 v[200:203], a[32:35], a[176:179], v[236:239]\n\t"
    "buffer_load_dwordx4 v5, s[40:43], s59 offen offset:1024 nt lds\n\t"
    "v_mfma_f32_16x16x32_bf16 v[200:203], a[36:39], a[180:183], v[200:203]\n\t"
    "buffer_load_dwordx4 v6, s[40:43], s59 offen offset:2048 nt lds\n\t"
    "v_mfma_f32_16x16x32_bf16 v[200:203], a[40:43], a[184:187], v[200:203]\n\t"
    "buffer_load_dwordx4 v7, s[40:43], s59 offen offset:3072 nt lds\n\t"
    "v_mfma_f32_16x16x32_bf16 v[200:203], a[44:47], a[188:191], v[200:203]\n\t"
    "ds_read_b128 a[32:35], v1 offset:24576\n\t"
    "ds_read_b128 a[4:7], v1 offset:16640\n\t"
    "v_mfma_f32_16x16x32_bf16 v[204:207], a[48:51], a[176:179], v[236:239]\n\t"
    "ds_read_b64_tr_b16 a[242:243], v3 offset:28672\n\t"
    "ds_read_b128 a[36:39], v1 offset:24832\n\t"
    "v_mfma_f32_16x16x32_bf16 v[204:207], a[52:55], a[180:183], v[204:207]\n\t"
    "ds_read_b128 a[16:19], v1 offset:20480\n\t"
    "ds_read_b128 a[44:47], v1 offset:25344\n\t"
    "v_mfma_f32_16x16x32_bf16 v[204:207], a[56:59], a[184:187], v[204:207]\n\t"
    "v_exp_f32 v193, v193\n\t"
    "v_mfma_f32_16x16x32_bf16 v[204:207], a[60:63], a[188:191], v[204:207]\n\t"
    "ds_read_b64_tr_b16 a[246:247], v3 offset:20480\n\t"
    "ds_read_b128 a[40:43], v1 offset:25088\n\t"
    "v_mfma_f32_16x16x32_bf16 v[80:83], v[208:211], a[64:67], 0\n\t"
    "v_exp_f32 v199, v199\n\t"
    "v_mfma_f32_16x16x32_bf16 v[80:83], v[212:215], a[68:71], v[80:83]\n\t"
    "v_exp_f32 v196, v196\n\t"
    "v_mfma_f32_16x16x32_bf16 v[84:87], v[208:211], a[72:75], 0\n\t"
    "s_add_u32 m0, s67, 114688\n\t"
    "ds_read_b128 a[0:3], v1 offset:16384\n\t"
    "v_mfma_f32_16x16x32_bf16 v[84:87], v[212:215], a[76:79], v[84:87]\n\t"
    "ds_read_b64_tr_b16 a[248:249], v2 offset:24576\n\t"
    "ds_read_b128 v[240:243], v14 offset:16384\n\t"
    "v_mfma_f32_16x16x32_bf16 v[88:91], v[208:211], a[80:83], 0\n\t"
    "ds_read_b64_tr_b16 a[254:255], v2 offset:20480\n\t"
    "s_add_u32 s59, s66, 49152\n\t"
    "v_mfma_f32_16x16x32_bf16 v[88:91], v[212:215], a[84:87], v[88:91]\n\t"
    "buffer_load_dwordx4 v4, s[44:47], s59 offen nt lds\n\t"
    "v_mfma_f32_16x16x32_bf16 v[92:95], v[208:211], a[88:91], 0\n\t"
    "buffer_load_dwordx4 v5, s[44:47], s59 offen offset:1024 nt lds\n\t"
    "v_mfma_f32_16x16x32_bf16 v[92:95], v[212:215], a[92:95], v[92:95]\n\t"
    "ds_read_b128 a[20:23], v1 offset:20736\n\t"
    "ds_read_b128 a[56:59], v1 offset:29184\n\t"
    "v_mfma_f32_16x16x32_bf16 v[96:99], v[208:211], a[96:99], 0\n\t"
    "v_exp_f32 v198, v198\n\t"
    "v_mfma_f32_16x16x32_bf16 v[96:99], v[212:215], a[100:103], v[96:99]\n\t"
    "ds_read_b64_tr_b16 a[250:251], v2 offset:28672\n\t"
    "ds_read_b128 a[28:31], v1 offset:21248\n\t"
    "v_mfma_f32_16x16x32_bf16 v[100:103], v[208:211], a[104:107], 0\n\t"
    "v_exp_f32 v203, v203\n\t"
    "v_mfma_f32_16x16x32_bf16 v[100:103], v[212:215], a[108:111], v[100:103]\n\t"
    "buffer_load_dwordx4 v4, s[44:47], s59 offen offset:2048 nt lds\n\t"
    "v_mfma_f32_16x16x32_bf16 v[104:107], v[208:211], a[112:115], 0\n\t"
    "ds_read_b128 a[8:11], v1 offset:16896\n\t"
    "ds_read_b64_tr_b16 a[232:233], v2 offset:24832\n\t"
    "v_mfma_f32_16x16x32_bf16 v[104:107], v[212:215], a[116:119], v[104:107]\n\t"
    "buffer_load_dwordx4 v5, s[44:47], s59 offen offset:3072 nt lds\n\t"
    "v_mfma_f32_16x16x32_bf16 v[108:111], v[208:211], a[120:123], 0\n\t"
    "ds_read_b128 a[24:27], v1 offset:20992\n\t"
    "ds_read_b128 a[60:63], v1 offset:29440\n\t"
    "v_mfma_f32_16x16x32_bf16 v[108:111], v[212:215], a[124:127], v[108:111]\n\t"
    "ds_read_b64_tr_b16 a[236:237], v2 offset:16640\n\t"
    "v_smfmac_f32_16x16x64_bf16 a[220:223], a[208:211], v[208:215], v0\n\t"
    "v_exp_f32 v206, v206\n\t"
    "v_mfma_f32_16x16x32_bf16 v[168:171], v[248:251], a[112:115], 0\n\t"
    "v_exp_f32 v207, v207\n\t"
    "v_mfma_f32_16x16x32_bf16 v[168:171], v[252:255], a[116:119], v[168:171]\n\t"
    "s_waitcnt lgkmcnt(10)\n\t"
    "ds_read_b128 a[48:51], v1 offset:28672\n\t"
    "v_mfma_f32_16x16x32_bf16 v[172:175], v[248:251], a[120:123], 0\n\t"
    "v_cvt_pk_bf16_f32 v223, v206, v207\n\t"
    "v_mfma_f32_16x16x32_bf16 v[172:175], v[252:255], a[124:127], v[172:175]\n\t"
    "ds_read_b64_tr_b16 a[252:253], v2 offset:16384\n\t"
    "v_mfma_f32_16x16x32_bf16 a[192:195], v[240:243], a[96:99], 0\n\t"
    "ds_read_b64_tr_b16 a[238:239], v2 offset:20736\n\t"
    "v_mfma_f32_16x16x32_bf16 a[192:195], v[244:247], a[100:103], a[192:195]\n\t"
    "ds_read_b64_tr_b16 a[244:245], v3 offset:16384\n\t"
    "v_mfma_f32_16x16x32_bf16 a[196:199], v[240:243], a[104:107], 0\n\t"
    "v_exp_f32 v205, v205\n\t"
    "v_mfma_f32_16x16x32_bf16 a[196:199], v[244:247], a[108:111], a[196:199]\n\t"
    "ds_read_b128 a[12:15], v1 offset:17152\n\t"
    "ds_read_b128 a[52:55], v1 offset:28928\n\t"
    "v_mfma_f32_16x16x32_bf16 a[200:203], v[240:243], a[112:115], 0\n\t"
    "v_exp_f32 v194, v194\n\t"
    "v_mfma_f32_16x16x32_bf16 a[200:203], v[244:247], a[116:119], a[200:203]\n\t"
    "v_exp_f32 v201, v201\n\t"
    "v_mfma_f32_16x16x32_bf16 a[204:207], v[240:243], a[120:123], 0\n\t"
    "ds_read_b64_tr_b16 a[234:235], v2 offset:28928\n\t"
    "v_mfma_f32_16x16x32_bf16 a[204:207], v[244:247], a[124:127], a[204:207]\n\t"
    "ds_read_b64_tr_b16 a[240:241], v3 offset:24576\n\t"
    "s_waitcnt lgkmcnt(3)\n\t"
    "s_mov_b32 s62, 14\n\t"
    "LOOP4_%=:\n\t"
    "v_mfma_f32_16x16x32_bf16 v[176:179], a[0:3], a[128:131], v[224:227]\n\t"
    "v_exp_f32 v197, v197\n\t"
    "v_mfma_f32_16x16x32_bf16 v[176:179], a[4:7], a[132:135], v[176:179]\n\t"
    "v_exp_f32 v192, v192\n\t"
    "v_mfma_f32_16x16x32_bf16 v[176:179], a[8:11], a[136:139], v[176:179]\n\t"
    "v_cvt_pk_bf16_f32 v216, v192, v193\n\t"
    "v_cvt_pk_bf16_f32 v218, v196, v197\n\t"
    "v_mfma_f32_16x16x32_bf16 v[176:179], a[12:15], a[140:143], v[176:179]\n\t"
    "v_exp_f32 v204, v204\n\t"
    "v_mfma_f32_16x16x32_bf16 v[180:183], a[16:19], a[128:131], v[224:227]\n\t"
    "v_exp_f32 v200, v200\n\t"
    "v_mfma_f32_16x16x32_bf16 v[180:183], a[20:23], a[132:135], v[180:183]\n\t"
    "s_waitcnt lgkmcnt(0)\n\t"
    "v_mfma_f32_16x16x32_bf16 v[180:183], a[24:27], a[136:139], v[180:183]\n\t"
    "v_exp_f32 v202, v202\n\t"
    "v_mfma_f32_16x16x32_bf16 v[180:183], a[28:31], a[140:143], v[180:183]\n\t"
    "v_exp_f32 v195, v195\n\t"
    "v_mfma_f32_16x16x32_bf16 v[184:187], a[32:35], a[128:131], v[224:227]\n\t"
    "v_cvt_pk_bf16_f32 v217, v194, v195\n\t"
    "v_cvt_pk_bf16_f32 v222, v204, v205\n\t"
    "v_mfma_f32_16x16x32_bf16 v[184:187], a[36:39], a[132:135], v[184:187]\n\t"
    "v_cvt_pk_bf16_f32 v219, v198, v199\n\t"
    "v_cvt_pk_bf16_f32 v220, v200, v201\n\t"
    "v_mfma_f32_16x16x32_bf16 v[184:187], a[40:43], a[136:139], v[184:187]\n\t"
    "v_exp_f32 v176, v176\n\t"
    "v_mfma_f32_16x16x32_bf16 v[184:187], a[44:47], a[140:143], v[184:187]\n\t"
    "v_exp_f32 v177, v177\n\t"
    "v_mfma_f32_16x16x32_bf16 v[188:191], a[48:51], a[128:131], v[224:227]\n\t"
    "v_cvt_pk_bf16_f32 v221, v202, v203\n\t"
    "v_cvt_pk_bf16_f32 v208, v176, v177\n\t"
    "v_mfma_f32_16x16x32_bf16 v[188:191], a[52:55], a[132:135], v[188:191]\n\t"
    "v_exp_f32 v179, v179\n\t"
    "v_mfma_f32_16x16x32_bf16 v[188:191], a[56:59], a[136:139], v[188:191]\n\t"
    "v_exp_f32 v182, v182\n\t"
    "v_mfma_f32_16x16x32_bf16 v[188:191], a[60:63], a[140:143], v[188:191]\n\t"
    "v_exp_f32 v178, v178\n\t"
    "v_mfma_f32_16x16x32_bf16 v[124:127], v[216:219], a[88:91], v[124:127]\n\t"
    "ds_read_b64_tr_b16 a[90:91], v3 offset:20736\n\t"
    "v_mfma_f32_16x16x32_bf16 v[124:127], v[220:223], a[92:95], v[124:127]\n\t"
    "v_exp_f32 v181, v181\n\t"
    "v_mfma_f32_16x16x32_bf16 v[128:131], v[216:219], a[96:99], v[128:131]\n\t"
    "ds_read_b64_tr_b16 a[88:89], v3 offset:16640\n\t"
    "ds_read_b64_tr_b16 a[94:95], v3 offset:28928\n\t"
    "v_mfma_f32_16x16x32_bf16 v[128:131], v[220:223], a[100:103], v[128:131]\n\t"
    "ds_read_b64_tr_b16 a[100:101], v2 offset:25088\n\t"
    "v_mfma_f32_16x16x32_bf16 v[132:135], v[216:219], a[104:107], v[132:135]\n\t"
    "ds_read_b64_tr_b16 a[98:99], v2 offset:20992\n\t"
    "v_mfma_f32_16x16x32_bf16 v[132:135], v[220:223], a[108:111], v[132:135]\n\t"
    "ds_read_b64_tr_b16 a[92:93], v3 offset:24832\n\t"
    "ds_read_b64_tr_b16 a[106:107], v3 offset:20992\n\t"
    "v_mfma_f32_16x16x32_bf16 v[136:139], v[216:219], a[112:115], v[136:139]\n\t"
    "v_exp_f32 v180, v180\n\t"
    "v_mfma_f32_16x16x32_bf16 v[136:139], v[220:223], a[116:119], v[136:139]\n\t"
    "ds_read_b64_tr_b16 a[110:111], v3 offset:29184\n\t"
    "ds_read_b64_tr_b16 a[118:119], v2 offset:29440\n\t"
    "v_mfma_f32_16x16x32_bf16 v[140:143], v[216:219], a[120:123], v[140:143]\n\t"
    "ds_read_b64_tr_b16 a[96:97], v2 offset:16896\n\t"
    "ds_read_b64_tr_b16 a[102:103], v2 offset:29184\n\t"
    "v_mfma_f32_16x16x32_bf16 v[140:143], v[220:223], a[124:127], v[140:143]\n\t"
    "ds_read_b64_tr_b16 a[114:115], v2 offset:21248\n\t"
    "v_mfma_f32_16x16x32_bf16 v[112:115], v[216:219], a[64:67], v[112:115]\n\t"
    "ds_read_b64_tr_b16 a[108:109], v3 offset:25088\n\t"
    "ds_read_b64_tr_b16 a[120:121], v3 offset:17152\n\t"
    "v_mfma_f32_16x16x32_bf16 v[112:115], v[220:223], a[68:71], v[112:115]\n\t"
    "v_exp_f32 v185, v185\n\t"
    "v_mfma_f32_16x16x32_bf16 v[116:119], v[216:219], a[72:75], v[116:119]\n\t"
    "ds_read_b64_tr_b16 a[112:113], v2 offset:17152\n\t"
    "ds_read_b64_tr_b16 a[116:117], v2 offset:25344\n\t"
    "v_mfma_f32_16x16x32_bf16 v[116:119], v[220:223], a[76:79], v[116:119]\n\t"
    "ds_read_b64_tr_b16 a[124:125], v3 offset:25344\n\t"
    "ds_read_b64_tr_b16 a[126:127], v3 offset:29440\n\t"
    "v_mfma_f32_16x16x32_bf16 v[120:123], v[216:219], a[80:83], v[120:123]\n\t"
    "v_exp_f32 v188, v188\n\t"
    "v_mfma_f32_16x16x32_bf16 v[120:123], v[220:223], a[84:87], v[120:123]\n\t"
    "ds_read_b64_tr_b16 a[104:105], v3 offset:16896\n\t"
    "ds_read_b64_tr_b16 a[122:123], v3 offset:21248\n\t"
    "v_smfmac_f32_16x16x64_bf16 a[224:227], a[208:211], v[216:223], v0\n\t"
    "v_mfma_f32_16x16x32_bf16 v[192:195], a[0:3], a[144:147], v[228:231]\n\t"
    "v_exp_f32 v190, v190\n\t"
    "v_mfma_f32_16x16x32_bf16 v[192:195], a[4:7], a[148:151], v[192:195]\n\t"
    "v_exp_f32 v191, v191\n\t"
    "v_mfma_f32_16x16x32_bf16 v[192:195], a[8:11], a[152:155], v[192:195]\n\t"
    "v_exp_f32 v187, v187\n\t"
    "v_mfma_f32_16x16x32_bf16 v[192:195], a[12:15], a[156:159], v[192:195]\n\t"
    "v_exp_f32 v184, v184\n\t"
    "v_mfma_f32_16x16x32_bf16 v[196:199], a[16:19], a[144:147], v[228:231]\n\t"
    "v_cvt_pk_bf16_f32 v209, v178, v179\n\t"
    "v_cvt_pk_bf16_f32 v212, v184, v185\n\t"
    "v_mfma_f32_16x16x32_bf16 v[196:199], a[20:23], a[148:151], v[196:199]\n\t"
    "v_exp_f32 v189, v189\n\t"
    "v_mfma_f32_16x16x32_bf16 v[196:199], a[24:27], a[152:155], v[196:199]\n\t"
    "v_exp_f32 v186, v186\n\t"
    "v_mfma_f32_16x16x32_bf16 v[196:199], a[28:31], a[156:159], v[196:199]\n\t"
    "v_cvt_pk_bf16_f32 v214, v188, v189\n\t"
    "v_cvt_pk_bf16_f32 v215, v190, v191\n\t"
    "v_mfma_f32_16x16x32_bf16 v[200:203], a[32:35], a[144:147], v[228:231]\n\t"
    "v_exp_f32 v183, v183\n\t"
    "v_mfma_f32_16x16x32_bf16 v[200:203], a[36:39], a[148:151], v[200:203]\n\t"
    "v_cvt_pk_bf16_f32 v211, v182, v183\n\t"
    "v_mfma_f32_16x16x32_bf16 v[200:203], a[40:43], a[152:155], v[200:203]\n\t"
    "v_exp_f32 v194, v194\n\t"
    "v_mfma_f32_16x16x32_bf16 v[200:203], a[44:47], a[156:159], v[200:203]\n\t"
    "v_exp_f32 v192, v192\n\t"
    "v_mfma_f32_16x16x32_bf16 v[204:207], a[48:51], a[144:147], v[228:231]\n\t"
    "v_cvt_pk_bf16_f32 v210, v180, v181\n\t"
    "v_cvt_pk_bf16_f32 v213, v186, v187\n\t"
    "v_mfma_f32_16x16x32_bf16 v[204:207], a[52:55], a[148:151], v[204:207]\n\t"
    "s_waitcnt lgkmcnt(15)\n\t"
    "v_xor_b32 v1, 0x8000, v1\n\t"
    "v_mfma_f32_16x16x32_bf16 v[204:207], a[56:59], a[152:155], v[204:207]\n\t"
    "v_exp_f32 v198, v198\n\t"
    "v_mfma_f32_16x16x32_bf16 v[204:207], a[60:63], a[156:159], v[204:207]\n\t"
    "v_exp_f32 v196, v196\n\t"
    "v_mfma_f32_16x16x32_bf16 v[16:19], v[208:211], a[252:255], v[16:19]\n\t"
    "v_exp_f32 v197, v197\n\t"
    "v_mfma_f32_16x16x32_bf16 v[16:19], v[212:215], a[248:251], v[16:19]\n\t"
    "v_exp_f32 v199, v199\n\t"
    "v_mfma_f32_16x16x32_bf16 v[20:23], v[208:211], a[244:247], v[20:23]\n\t"
    "v_exp_f32 v193, v193\n\t"
    "v_mfma_f32_16x16x32_bf16 v[20:23], v[212:215], a[240:243], v[20:23]\n\t"
    "v_exp_f32 v203, v203\n\t"
    "v_mfma_f32_16x16x32_bf16 v[24:27], v[208:211], a[236:239], v[24:27]\n\t"
    "v_exp_f32 v202, v202\n\t"
    "v_mfma_f32_16x16x32_bf16 v[24:27], v[212:215], a[232:235], v[24:27]\n\t"
    "s_waitcnt lgkmcnt(1)\n\t"
    "v_cvt_pk_bf16_f32 v219, v198, v199\n\t"
    "v_mfma_f32_16x16x32_bf16 v[28:31], v[208:211], a[88:91], v[28:31]\n\t"
    "v_exp_f32 v201, v201\n\t"
    "v_mfma_f32_16x16x32_bf16 v[28:31], v[212:215], a[92:95], v[28:31]\n\t"
    "v_exp_f32 v206, v206\n\t"
    "v_mfma_f32_16x16x32_bf16 v[32:35], v[208:211], a[96:99], v[32:35]\n\t"
    "v_exp_f32 v204, v204\n\t"
    "v_mfma_f32_16x16x32_bf16 v[32:35], v[212:215], a[100:103], v[32:35]\n\t"
    "s_waitcnt lgkmcnt(0)\n\t"
    "v_cvt_pk_bf16_f32 v221, v202, v203\n\t"
    "v_mfma_f32_16x16x32_bf16 v[36:39], v[208:211], a[104:107], v[36:39]\n\t"
    "v_exp_f32 v195, v195\n\t"
    "v_mfma_f32_16x16x32_bf16 v[36:39], v[212:215], a[108:111], v[36:39]\n\t"
    "v_exp_f32 v200, v200\n\t"
    "v_mfma_f32_16x16x32_bf16 v[40:43], v[208:211], a[112:115], v[40:43]\n\t"
    "v_exp_f32 v207, v207\n\t"
    "v_mfma_f32_16x16x32_bf16 v[40:43], v[212:215], a[116:119], v[40:43]\n\t"
    "v_cvt_pk_bf16_f32 v216, v192, v193\n\t"
    "v_xor_b32 v2, 0x8000, v2\n\t"
    "v_mfma_f32_16x16x32_bf16 v[44:47], v[208:211], a[120:123], v[44:47]\n\t"
    "v_exp_f32 v205, v205\n\t"
    "v_mfma_f32_16x16x32_bf16 v[44:47], v[212:215], a[124:127], v[44:47]\n\t"
    "v_cvt_pk_bf16_f32 v218, v196, v197\n\t"
    "v_xor_b32 v3, 0x8000, v3\n\t"
    "v_smfmac_f32_16x16x64_bf16 a[212:215], a[208:211], v[208:215], v0\n\t"
    "v_cvt_pk_bf16_f32 v222, v204, v205\n\t"
    "s_cmp_eq_u32 s62, 14\n\t"
    "s_cbranch_scc0 SKIPW_%=\n\t"
    "s_waitcnt vmcnt(8)\n\t"
    "SKIPW_%=:\n\t"
    "s_waitcnt vmcnt(32)\n\t"
    "s_barrier\n\t"
    "v_mfma_f32_16x16x32_bf16 v[176:179], a[0:3], a[160:163], v[232:235]\n\t"
    "ds_read_b64_tr_b16 a[74:75], v3 offset:4096\n\t"
    "s_xor_b32 s56, s56, 0x8000\n\t"
    "v_mfma_f32_16x16x32_bf16 v[176:179], a[4:7], a[164:167], v[176:179]\n\t"
    "ds_read_b64_tr_b16 a[64:65], v2 offset:0\n\t"
    "s_add_u32 m0, s56, 0\n\t"
    "v_mfma_f32_16x16x32_bf16 v[176:179], a[8:11], a[168:171], v[176:179]\n\t"
    "ds_read_b64_tr_b16 a[76:77], v3 offset:8192\n\t"
    "ds_read_b64_tr_b16 a[86:87], v2 offset:12544\n\t"
    "v_mfma_f32_16x16x32_bf16 v[176:179], a[12:15], a[172:175], v[176:179]\n\t"
    "ds_read_b128 v[252:255], v14 offset:11264\n\t"
    "buffer_load_dwordx4 v4, s[40:43], s57 offen nt lds\n\t"
    "v_mfma_f32_16x16x32_bf16 v[180:183], a[16:19], a[160:163], v[232:235]\n\t"
    "buffer_load_dwordx4 v5, s[40:43], s57 offen offset:1024 nt lds\n\t"
    "v_mfma_f32_16x16x32_bf16 v[180:183], a[20:23], a[164:167], v[180:183]\n\t"
    "ds_read_b64_tr_b16 a[66:67], v2 offset:4096\n\t"
    "ds_read_b128 v[244:247], v14 offset:3072\n\t"
    "v_mfma_f32_16x16x32_bf16 v[180:183], a[24:27], a[168:171], v[180:183]\n\t"
    "ds_read_b64_tr_b16 a[80:81], v2 offset:256\n\t"
    "buffer_load_dwordx4 v6, s[40:43], s57 offen offset:2048 nt lds\n\t"
    "v_mfma_f32_16x16x32_bf16 v[180:183], a[28:31], a[172:175], v[180:183]\n\t"
    "ds_read_b128 v[240:243], v14 offset:2048\n\t"
    "buffer_load_dwordx4 v7, s[40:43], s57 offen offset:3072 nt lds\n\t"
    "v_mfma_f32_16x16x32_bf16 v[184:187], a[32:35], a[160:163], v[232:235]\n\t"
    "v_cvt_pk_bf16_f32 v217, v194, v195\n\t"
    "v_cvt_pk_bf16_f32 v220, v200, v201\n\t"
    "v_mfma_f32_16x16x32_bf16 v[184:187], a[36:39], a[164:167], v[184:187]\n\t"
    "v_cvt_pk_bf16_f32 v223, v206, v207\n\t"
    "s_add_u32 m0, s56, 4096\n\t"
    "v_mfma_f32_16x16x32_bf16 v[184:187], a[40:43], a[168:171], v[184:187]\n\t"
    "ds_read_b64_tr_b16 a[72:73], v3 offset:0\n\t"
    "ds_read_b64_tr_b16 a[82:83], v2 offset:4352\n\t"
    "v_mfma_f32_16x16x32_bf16 v[184:187], a[44:47], a[172:175], v[184:187]\n\t"
    "buffer_load_dwordx4 v4, s[40:43], s63 offen nt lds\n\t"
    "v_mfma_f32_16x16x32_bf16 v[188:191], a[48:51], a[160:163], v[232:235]\n\t"
    "v_exp_f32 v177, v177\n\t"
    "v_mfma_f32_16x16x32_bf16 v[188:191], a[52:55], a[164:167], v[188:191]\n\t"
    "buffer_load_dwordx4 v5, s[40:43], s63 offen offset:1024 nt lds\n\t"
    "v_mfma_f32_16x16x32_bf16 v[188:191], a[56:59], a[168:171], v[188:191]\n\t"
    "buffer_load_dwordx4 v6, s[40:43], s63 offen offset:2048 nt lds\n\t"
    "v_mfma_f32_16x16x32_bf16 v[188:191], a[60:63], a[172:175], v[188:191]\n\t"
    "ds_read_b64_tr_b16 a[70:71], v2 offset:12288\n\t"
    "ds_read_b64_tr_b16 a[78:79], v3 offset:12288\n\t"
    "v_mfma_f32_16x16x32_bf16 v[48:51], v[216:219], a[252:255], v[48:51]\n\t"
    "v_exp_f32 v183, v183\n\t"
    "v_mfma_f32_16x16x32_bf16 v[48:51], v[220:223], a[248:251], v[48:51]\n\t"
    "buffer_load_dwordx4 v7, s[40:43], s63 offen offset:3072 nt lds\n\t"
    "v_mfma_f32_16x16x32_bf16 v[52:55], v[216:219], a[244:247], v[52:55]\n\t"
    "ds_read_b64_tr_b16 a[84:85], v2 offset:8448\n\t"
    "s_add_u32 m0, s56, 8192\n\t"
    "v_mfma_f32_16x16x32_bf16 v[52:55], v[220:223], a[240:243], v[52:55]\n\t"
    "buffer_load_dwordx4 v4, s[40:43], s64 offen nt lds\n\t"
    "v_mfma_f32_16x16x32_bf16 v[56:59], v[216:219], a[236:239], v[56:59]\n\t"
    "buffer_load_dwordx4 v5, s[40:43], s64 offen offset:1024 nt lds\n\t"
    "v_mfma_f32_16x16x32_bf16 v[56:59], v[220:223], a[232:235], v[56:59]\n\t"
    "buffer_load_dwordx4 v6, s[40:43], s64 offen offset:2048 nt lds\n\t"
    "v_mfma_f32_16x16x32_bf16 v[60:63], v[216:219], a[88:91], v[60:63]\n\t"
    "v_exp_f32 v182, v182\n\t"
    "v_mfma_f32_16x16x32_bf16 v[60:63], v[220:223], a[92:95], v[60:63]\n\t"
    "v_exp_f32 v189, v189\n\t"
    "v_mfma_f32_16x16x32_bf16 v[64:67], v[216:219], a[96:99], v[64:67]\n\t"
    "buffer_load_dwordx4 v7, s[40:43], s64 offen offset:3072 nt lds\n\t"
    "v_mfma_f32_16x16x32_bf16 v[64:67], v[220:223], a[100:103], v[64:67]\n\t"
    "ds_read_b128 v[248:251], v14 offset:10240\n\t"
    "s_add_u32 m0, s56, 12288\n\t"
    "v_mfma_f32_16x16x32_bf16 v[68:71], v[216:219], a[104:107], v[68:71]\n\t"
    "buffer_load_dwordx4 v4, s[40:43], s65 offen nt lds\n\t"
    "v_mfma_f32_16x16x32_bf16 v[68:71], v[220:223], a[108:111], v[68:71]\n\t"
    "v_exp_f32 v187, v187\n\t"
    "v_mfma_f32_16x16x32_bf16 v[72:75], v[216:219], a[112:115], v[72:75]\n\t"
    "buffer_load_dwordx4 v5, s[40:43], s65 offen offset:1024 nt lds\n\t"
    "v_mfma_f32_16x16x32_bf16 v[72:75], v[220:223], a[116:119], v[72:75]\n\t"
    "v_exp_f32 v181, v181\n\t"
    "v_mfma_f32_16x16x32_bf16 v[76:79], v[216:219], a[120:123], v[76:79]\n\t"
    "buffer_load_dwordx4 v6, s[40:43], s65 offen offset:2048 nt lds\n\t"
    "v_mfma_f32_16x16x32_bf16 v[76:79], v[220:223], a[124:127], v[76:79]\n\t"
    "buffer_load_dwordx4 v7, s[40:43], s65 offen offset:3072 nt lds\n\t"
    "v_smfmac_f32_16x16x64_bf16 a[216:219], a[208:211], v[216:223], v0\n\t"
    "s_waitcnt lgkmcnt(6)\n\t"
    "v_mfma_f32_16x16x32_bf16 v[144:147], v[240:243], a[96:99], v[144:147]\n\t"
    "v_exp_f32 v186, v186\n\t"
    "v_mfma_f32_16x16x32_bf16 v[144:147], v[244:247], a[100:103], v[144:147]\n\t"
    "v_exp_f32 v180, v180\n\t"
    "v_mfma_f32_16x16x32_bf16 v[148:151], v[240:243], a[104:107], v[148:151]\n\t"
    "ds_read_b64_tr_b16 a[68:69], v2 offset:8192\n\t"
    "s_add_u32 m0, s56, 65536\n\t"
    "v_mfma_f32_16x16x32_bf16 v[148:151], v[244:247], a[108:111], v[148:151]\n\t"
    "buffer_load_dwordx4 v4, s[44:47], s57 offen nt lds\n\t"
    "v_mfma_f32_16x16x32_bf16 v[152:155], v[240:243], a[112:115], v[152:155]\n\t"
    "v_exp_f32 v184, v184\n\t"
    "v_mfma_f32_16x16x32_bf16 v[152:155], v[244:247], a[116:119], v[152:155]\n\t"
    "buffer_load_dwordx4 v5, s[44:47], s57 offen offset:1024 nt lds\n\t"
    "v_mfma_f32_16x16x32_bf16 v[156:159], v[240:243], a[120:123], v[156:159]\n\t"
    "v_exp_f32 v178, v178\n\t"
    "v_mfma_f32_16x16x32_bf16 v[156:159], v[244:247], a[124:127], v[156:159]\n\t"
    "s_waitcnt lgkmcnt(1)\n\t"
    "buffer_load_dwordx4 v4, s[44:47], s57 offen offset:2048 nt lds\n\t"
    "v_mfma_f32_16x16x32_bf16 v[160:163], v[248:251], a[96:99], v[160:163]\n\t"
    "buffer_load_dwordx4 v5, s[44:47], s57 offen offset:3072 nt lds\n\t"
    "v_mfma_f32_16x16x32_bf16 v[160:163], v[252:255], a[100:103], v[160:163]\n\t"
    "v_cvt_pk_bf16_f32 v210, v180, v181\n\t"
    "s_add_u32 m0, s56, 69632\n\t"
    "v_mfma_f32_16x16x32_bf16 v[164:167], v[248:251], a[104:107], v[164:167]\n\t"
    "buffer_load_dwordx4 v4, s[44:47], s63 offen nt lds\n\t"
    "v_mfma_f32_16x16x32_bf16 v[164:167], v[252:255], a[108:111], v[164:167]\n\t"
    "v_exp_f32 v188, v188\n\t"
    "v_mfma_f32_16x16x32_bf16 v[192:195], a[0:3], a[176:179], v[236:239]\n\t"
    "buffer_load_dwordx4 v5, s[44:47], s63 offen offset:1024 nt lds\n\t"
    "v_mfma_f32_16x16x32_bf16 v[192:195], a[4:7], a[180:183], v[192:195]\n\t"
    "buffer_load_dwordx4 v4, s[44:47], s63 offen offset:2048 nt lds\n\t"
    "v_mfma_f32_16x16x32_bf16 v[192:195], a[8:11], a[184:187], v[192:195]\n\t"
    "v_exp_f32 v185, v185\n\t"
    "v_mfma_f32_16x16x32_bf16 v[192:195], a[12:15], a[188:191], v[192:195]\n\t"
    "v_exp_f32 v191, v191\n\t"
    "v_mfma_f32_16x16x32_bf16 v[196:199], a[16:19], a[176:179], v[236:239]\n\t"
    "v_exp_f32 v176, v176\n\t"
    "v_mfma_f32_16x16x32_bf16 v[196:199], a[20:23], a[180:183], v[196:199]\n\t"
    "v_cvt_pk_bf16_f32 v211, v182, v183\n\t"
    "v_cvt_pk_bf16_f32 v214, v188, v189\n\t"
    "v_mfma_f32_16x16x32_bf16 v[196:199], a[24:27], a[184:187], v[196:199]\n\t"
    "buffer_load_dwordx4 v5, s[44:47], s63 offen offset:3072 nt lds\n\t"
    "v_mfma_f32_16x16x32_bf16 v[196:199], a[28:31], a[188:191], v[196:199]\n\t"
    "v_cvt_pk_bf16_f32 v213, v186, v187\n\t"
    "s_add_u32 m0, s56, 73728\n\t"
    "v_mfma_f32_16x16x32_bf16 v[200:203], a[32:35], a[176:179], v[236:239]\n\t"
    "buffer_load_dwordx4 v4, s[44:47], s64 offen nt lds\n\t"
    "v_mfma_f32_16x16x32_bf16 v[200:203], a[36:39], a[180:183], v[200:203]\n\t"
    "buffer_load_dwordx4 v5, s[44:47], s64 offen offset:1024 nt lds\n\t"
    "v_mfma_f32_16x16x32_bf16 v[200:203], a[40:43], a[184:187], v[200:203]\n\t"
    "v_exp_f32 v179, v179\n\t"
    "v_mfma_f32_16x16x32_bf16 v[200:203], a[44:47], a[188:191], v[200:203]\n\t"
    "v_exp_f32 v190, v190\n\t"
    "v_mfma_f32_16x16x32_bf16 v[204:207], a[48:51], a[176:179], v[236:239]\n\t"
    "v_cvt_pk_bf16_f32 v208, v176, v177\n\t"
    "v_cvt_pk_bf16_f32 v209, v178, v179\n\t"
    "v_mfma_f32_16x16x32_bf16 v[204:207], a[52:55], a[180:183], v[204:207]\n\t"
    "buffer_load_dwordx4 v4, s[44:47], s64 offen offset:2048 nt lds\n\t"
    "v_mfma_f32_16x16x32_bf16 v[204:207], a[56:59], a[184:187], v[204:207]\n\t"
    "v_cvt_pk_bf16_f32 v212, v184, v185\n\t"
    "v_cvt_pk_bf16_f32 v215, v190, v191\n\t"
    "v_mfma_f32_16x16x32_bf16 v[204:207], a[60:63], a[188:191], v[204:207]\n\t"
    "buffer_load_dwordx4 v5, s[44:47], s64 offen offset:3072 nt lds\n\t"
    "v_mfma_f32_16x16x32_bf16 v[80:83], v[208:211], a[252:255], v[80:83]\n\t"
    "ds_read_b128 a[8:11], v1 offset:512\n\t"
    "s_add_u32 m0, s56, 77824\n\t"
    "v_mfma_f32_16x16x32_bf16 v[80:83], v[212:215], a[248:251], v[80:83]\n\t"
    "buffer_load_dwordx4 v4, s[44:47], s65 offen nt lds\n\t"
    "v_mfma_f32_16x16x32_bf16 v[84:87], v[208:211], a[244:247], v[84:87]\n\t"
    "buffer_load_dwordx4 v5, s[44:47], s65 offen offset:1024 nt lds\n\t"
    "v_mfma_f32_16x16x32_bf16 v[84:87], v[212:215], a[240:243], v[84:87]\n\t"
    "ds_read_b128 a[24:27], v1 offset:4608\n\t"
    "ds_read_b128 a[32:35], v1 offset:8192\n\t"
    "v_mfma_f32_16x16x32_bf16 v[88:91], v[208:211], a[236:239], v[88:91]\n\t"
    "buffer_load_dwordx4 v4, s[44:47], s65 offen offset:2048 nt lds\n\t"
    "v_mfma_f32_16x16x32_bf16 v[88:91], v[212:215], a[232:235], v[88:91]\n\t"
    "ds_read_b128 a[16:19], v1 offset:4096\n\t"
    "ds_read_b128 a[40:43], v1 offset:8704\n\t"
    "v_mfma_f32_16x16x32_bf16 v[92:95], v[208:211], a[88:91], v[92:95]\n\t"
    "v_exp_f32 v196, v196\n\t"
    "v_mfma_f32_16x16x32_bf16 v[92:95], v[212:215], a[92:95], v[92:95]\n\t"
    "ds_read_b128 a[4:7], v1 offset:256\n\t"
    "ds_read_b128 a[36:39], v1 offset:8448\n\t"
    "v_mfma_f32_16x16x32_bf16 v[96:99], v[208:211], a[96:99], v[96:99]\n\t"
    "ds_read_b128 a[20:23], v1 offset:4352\n\t"
    "ds_read_b128 v[240:243], v14 offset:18432\n\t"
    "v_mfma_f32_16x16x32_bf16 v[96:99], v[212:215], a[100:103], v[96:99]\n\t"
    "v_exp_f32 v205, v205\n\t"
    "v_mfma_f32_16x16x32_bf16 v[100:103], v[208:211], a[104:107], v[100:103]\n\t"
    "v_exp_f32 v194, v194\n\t"
    "v_mfma_f32_16x16x32_bf16 v[100:103], v[212:215], a[108:111], v[100:103]\n\t"
    "ds_read_b128 a[12:15], v1 offset:768\n\t"
    "s_waitcnt vmcnt(62)\n\t"
    "v_mfma_f32_16x16x32_bf16 v[104:107], v[208:211], a[112:115], v[104:107]\n\t"
    "ds_read_b128 a[0:3], v1 offset:0\n\t"
    "ds_read_b128 v[244:247], v14 offset:19456\n\t"
    "v_mfma_f32_16x16x32_bf16 v[104:107], v[212:215], a[116:119], v[104:107]\n\t"
    "buffer_load_dwordx4 v5, s[44:47], s65 offen offset:3072 nt lds\n\t"
    "v_mfma_f32_16x16x32_bf16 v[108:111], v[208:211], a[120:123], v[108:111]\n\t"
    "v_exp_f32 v199, v199\n\t"
    "v_mfma_f32_16x16x32_bf16 v[108:111], v[212:215], a[124:127], v[108:111]\n\t"
    "ds_read_b128 a[28:31], v1 offset:4864\n\t"
    "ds_read_b128 a[56:59], v1 offset:12800\n\t"
    "v_smfmac_f32_16x16x64_bf16 a[220:223], a[208:211], v[208:215], v0\n\t"
    "v_mfma_f32_16x16x32_bf16 v[168:171], v[248:251], a[112:115], v[168:171]\n\t"
    "ds_read_b128 a[60:63], v1 offset:13056\n\t"
    "v_mfma_f32_16x16x32_bf16 v[168:171], v[252:255], a[116:119], v[168:171]\n\t"
    "s_waitcnt lgkmcnt(3)\n\t"
    "s_add_u32 s57, s57, 16384\n\t"
    "v_mfma_f32_16x16x32_bf16 v[172:175], v[248:251], a[120:123], v[172:175]\n\t"
    "ds_read_b128 a[44:47], v1 offset:8960\n\t"
    "s_add_u32 s63, s63, 16384\n\t"
    "v_mfma_f32_16x16x32_bf16 v[172:175], v[252:255], a[124:127], v[172:175]\n\t"
    "v_exp_f32 v203, v203\n\t"
    "v_mfma_f32_16x16x32_bf16 a[192:195], v[240:243], a[96:99], a[192:195]\n\t"
    "ds_read_b128 a[48:51], v1 offset:12288\n\t"
    "s_add_u32 s64, s64, 16384\n\t"
    "v_mfma_f32_16x16x32_bf16 a[192:195], v[244:247], a[100:103], a[192:195]\n\t"
    "v_exp_f32 v206, v206\n\t"
    "v_mfma_f32_16x16x32_bf16 a[196:199], v[240:243], a[104:107], a[196:199]\n\t"
    "v_exp_f32 v193, v193\n\t"
    "v_mfma_f32_16x16x32_bf16 a[196:199], v[244:247], a[108:111], a[196:199]\n\t"
    "ds_read_b128 a[52:55], v1 offset:12544\n\t"
    "s_add_u32 s65, s65, 16384\n\t"
    "v_mfma_f32_16x16x32_bf16 a[200:203], v[240:243], a[112:115], a[200:203]\n\t"
    "v_exp_f32 v198, v198\n\t"
    "v_mfma_f32_16x16x32_bf16 a[200:203], v[244:247], a[116:119], a[200:203]\n\t"
    "v_exp_f32 v207, v207\n\t"
    "v_mfma_f32_16x16x32_bf16 a[204:207], v[240:243], a[120:123], a[204:207]\n\t"
    "v_exp_f32 v201, v201\n\t"
    "v_mfma_f32_16x16x32_bf16 a[204:207], v[244:247], a[124:127], a[204:207]\n\t"
    "v_cvt_pk_bf16_f32 v223, v206, v207\n\t"
    "s_waitcnt lgkmcnt(5)\n\t"
    "v_mfma_f32_16x16x32_bf16 v[176:179], a[0:3], a[128:131], v[224:227]\n\t"
    "v_exp_f32 v197, v197\n\t"
    "v_mfma_f32_16x16x32_bf16 v[176:179], a[4:7], a[132:135], v[176:179]\n\t"
    "v_exp_f32 v192, v192\n\t"
    "v_mfma_f32_16x16x32_bf16 v[176:179], a[8:11], a[136:139], v[176:179]\n\t"
    "v_cvt_pk_bf16_f32 v216, v192, v193\n\t"
    "v_cvt_pk_bf16_f32 v218, v196, v197\n\t"
    "v_mfma_f32_16x16x32_bf16 v[176:179], a[12:15], a[140:143], v[176:179]\n\t"
    "v_exp_f32 v204, v204\n\t"
    "v_mfma_f32_16x16x32_bf16 v[180:183], a[16:19], a[128:131], v[224:227]\n\t"
    "v_exp_f32 v200, v200\n\t"
    "v_mfma_f32_16x16x32_bf16 v[180:183], a[20:23], a[132:135], v[180:183]\n\t"
    "s_waitcnt lgkmcnt(0)\n\t"
    "v_mfma_f32_16x16x32_bf16 v[180:183], a[24:27], a[136:139], v[180:183]\n\t"
    "v_exp_f32 v202, v202\n\t"
    "v_mfma_f32_16x16x32_bf16 v[180:183], a[28:31], a[140:143], v[180:183]\n\t"
    "v_exp_f32 v195, v195\n\t"
    "v_mfma_f32_16x16x32_bf16 v[184:187], a[32:35], a[128:131], v[224:227]\n\t"
    "v_cvt_pk_bf16_f32 v217, v194, v195\n\t"
    "v_cvt_pk_bf16_f32 v222, v204, v205\n\t"
    "v_mfma_f32_16x16x32_bf16 v[184:187], a[36:39], a[132:135], v[184:187]\n\t"
    "v_cvt_pk_bf16_f32 v219, v198, v199\n\t"
    "v_cvt_pk_bf16_f32 v220, v200, v201\n\t"
    "v_mfma_f32_16x16x32_bf16 v[184:187], a[40:43], a[136:139], v[184:187]\n\t"
    "v_exp_f32 v176, v176\n\t"
    "v_mfma_f32_16x16x32_bf16 v[184:187], a[44:47], a[140:143], v[184:187]\n\t"
    "v_exp_f32 v177, v177\n\t"
    "v_mfma_f32_16x16x32_bf16 v[188:191], a[48:51], a[128:131], v[224:227]\n\t"
    "v_cvt_pk_bf16_f32 v221, v202, v203\n\t"
    "v_cvt_pk_bf16_f32 v208, v176, v177\n\t"
    "v_mfma_f32_16x16x32_bf16 v[188:191], a[52:55], a[132:135], v[188:191]\n\t"
    "v_exp_f32 v179, v179\n\t"
    "v_mfma_f32_16x16x32_bf16 v[188:191], a[56:59], a[136:139], v[188:191]\n\t"
    "v_exp_f32 v182, v182\n\t"
    "v_mfma_f32_16x16x32_bf16 v[188:191], a[60:63], a[140:143], v[188:191]\n\t"
    "v_exp_f32 v178, v178\n\t"
    "v_mfma_f32_16x16x32_bf16 v[124:127], v[216:219], a[88:91], v[124:127]\n\t"
    "ds_read_b64_tr_b16 a[90:91], v3 offset:4352\n\t"
    "v_mfma_f32_16x16x32_bf16 v[124:127], v[220:223], a[92:95], v[124:127]\n\t"
    "v_exp_f32 v181, v181\n\t"
    "v_mfma_f32_16x16x32_bf16 v[128:131], v[216:219], a[96:99], v[128:131]\n\t"
    "ds_read_b64_tr_b16 a[88:89], v3 offset:256\n\t"
    "ds_read_b64_tr_b16 a[94:95], v3 offset:12544\n\t"
    "v_mfma_f32_16x16x32_bf16 v[128:131], v[220:223], a[100:103], v[128:131]\n\t"
    "ds_read_b64_tr_b16 a[100:101], v2 offset:8704\n\t"
    "v_mfma_f32_16x16x32_bf16 v[132:135], v[216:219], a[104:107], v[132:135]\n\t"
    "ds_read_b64_tr_b16 a[98:99], v2 offset:4608\n\t"
    "v_mfma_f32_16x16x32_bf16 v[132:135], v[220:223], a[108:111], v[132:135]\n\t"
    "ds_read_b64_tr_b16 a[92:93], v3 offset:8448\n\t"
    "ds_read_b64_tr_b16 a[106:107], v3 offset:4608\n\t"
    "v_mfma_f32_16x16x32_bf16 v[136:139], v[216:219], a[112:115], v[136:139]\n\t"
    "v_exp_f32 v180, v180\n\t"
    "v_mfma_f32_16x16x32_bf16 v[136:139], v[220:223], a[116:119], v[136:139]\n\t"
    "ds_read_b64_tr_b16 a[110:111], v3 offset:12800\n\t"
    "ds_read_b64_tr_b16 a[118:119], v2 offset:13056\n\t"
    "v_mfma_f32_16x16x32_bf16 v[140:143], v[216:219], a[120:123], v[140:143]\n\t"
    "ds_read_b64_tr_b16 a[96:97], v2 offset:512\n\t"
    "ds_read_b64_tr_b16 a[102:103], v2 offset:12800\n\t"
    "v_mfma_f32_16x16x32_bf16 v[140:143], v[220:223], a[124:127], v[140:143]\n\t"
    "ds_read_b64_tr_b16 a[114:115], v2 offset:4864\n\t"
    "v_mfma_f32_16x16x32_bf16 v[112:115], v[216:219], a[252:255], v[112:115]\n\t"
    "ds_read_b64_tr_b16 a[108:109], v3 offset:8704\n\t"
    "ds_read_b64_tr_b16 a[120:121], v3 offset:768\n\t"
    "v_mfma_f32_16x16x32_bf16 v[112:115], v[220:223], a[248:251], v[112:115]\n\t"
    "v_exp_f32 v185, v185\n\t"
    "v_mfma_f32_16x16x32_bf16 v[116:119], v[216:219], a[244:247], v[116:119]\n\t"
    "ds_read_b64_tr_b16 a[112:113], v2 offset:768\n\t"
    "ds_read_b64_tr_b16 a[116:117], v2 offset:8960\n\t"
    "v_mfma_f32_16x16x32_bf16 v[116:119], v[220:223], a[240:243], v[116:119]\n\t"
    "ds_read_b64_tr_b16 a[124:125], v3 offset:8960\n\t"
    "ds_read_b64_tr_b16 a[126:127], v3 offset:13056\n\t"
    "v_mfma_f32_16x16x32_bf16 v[120:123], v[216:219], a[236:239], v[120:123]\n\t"
    "v_exp_f32 v188, v188\n\t"
    "v_mfma_f32_16x16x32_bf16 v[120:123], v[220:223], a[232:235], v[120:123]\n\t"
    "ds_read_b64_tr_b16 a[104:105], v3 offset:512\n\t"
    "ds_read_b64_tr_b16 a[122:123], v3 offset:4864\n\t"
    "v_smfmac_f32_16x16x64_bf16 a[224:227], a[208:211], v[216:223], v0\n\t"
    "v_mfma_f32_16x16x32_bf16 v[192:195], a[0:3], a[144:147], v[228:231]\n\t"
    "v_exp_f32 v190, v190\n\t"
    "v_mfma_f32_16x16x32_bf16 v[192:195], a[4:7], a[148:151], v[192:195]\n\t"
    "v_exp_f32 v191, v191\n\t"
    "v_mfma_f32_16x16x32_bf16 v[192:195], a[8:11], a[152:155], v[192:195]\n\t"
    "v_exp_f32 v187, v187\n\t"
    "v_mfma_f32_16x16x32_bf16 v[192:195], a[12:15], a[156:159], v[192:195]\n\t"
    "v_exp_f32 v184, v184\n\t"
    "v_mfma_f32_16x16x32_bf16 v[196:199], a[16:19], a[144:147], v[228:231]\n\t"
    "v_cvt_pk_bf16_f32 v209, v178, v179\n\t"
    "v_cvt_pk_bf16_f32 v212, v184, v185\n\t"
    "v_mfma_f32_16x16x32_bf16 v[196:199], a[20:23], a[148:151], v[196:199]\n\t"
    "v_exp_f32 v189, v189\n\t"
    "v_mfma_f32_16x16x32_bf16 v[196:199], a[24:27], a[152:155], v[196:199]\n\t"
    "v_exp_f32 v186, v186\n\t"
    "v_mfma_f32_16x16x32_bf16 v[196:199], a[28:31], a[156:159], v[196:199]\n\t"
    "v_cvt_pk_bf16_f32 v214, v188, v189\n\t"
    "v_cvt_pk_bf16_f32 v215, v190, v191\n\t"
    "v_mfma_f32_16x16x32_bf16 v[200:203], a[32:35], a[144:147], v[228:231]\n\t"
    "v_exp_f32 v183, v183\n\t"
    "v_mfma_f32_16x16x32_bf16 v[200:203], a[36:39], a[148:151], v[200:203]\n\t"
    "v_cvt_pk_bf16_f32 v211, v182, v183\n\t"
    "v_mfma_f32_16x16x32_bf16 v[200:203], a[40:43], a[152:155], v[200:203]\n\t"
    "v_exp_f32 v194, v194\n\t"
    "v_mfma_f32_16x16x32_bf16 v[200:203], a[44:47], a[156:159], v[200:203]\n\t"
    "v_exp_f32 v192, v192\n\t"
    "v_mfma_f32_16x16x32_bf16 v[204:207], a[48:51], a[144:147], v[228:231]\n\t"
    "v_cvt_pk_bf16_f32 v210, v180, v181\n\t"
    "v_cvt_pk_bf16_f32 v213, v186, v187\n\t"
    "v_mfma_f32_16x16x32_bf16 v[204:207], a[52:55], a[148:151], v[204:207]\n\t"
    "s_waitcnt lgkmcnt(15)\n\t"
    "v_mfma_f32_16x16x32_bf16 v[204:207], a[56:59], a[152:155], v[204:207]\n\t"
    "v_exp_f32 v198, v198\n\t"
    "v_mfma_f32_16x16x32_bf16 v[204:207], a[60:63], a[156:159], v[204:207]\n\t"
    "v_exp_f32 v196, v196\n\t"
    "v_mfma_f32_16x16x32_bf16 v[16:19], v[208:211], a[64:67], v[16:19]\n\t"
    "v_exp_f32 v197, v197\n\t"
    "v_mfma_f32_16x16x32_bf16 v[16:19], v[212:215], a[68:71], v[16:19]\n\t"
    "v_exp_f32 v199, v199\n\t"
    "v_mfma_f32_16x16x32_bf16 v[20:23], v[208:211], a[72:75], v[20:23]\n\t"
    "v_exp_f32 v193, v193\n\t"
    "v_mfma_f32_16x16x32_bf16 v[20:23], v[212:215], a[76:79], v[20:23]\n\t"
    "v_exp_f32 v203, v203\n\t"
    "v_mfma_f32_16x16x32_bf16 v[24:27], v[208:211], a[80:83], v[24:27]\n\t"
    "v_exp_f32 v202, v202\n\t"
    "v_mfma_f32_16x16x32_bf16 v[24:27], v[212:215], a[84:87], v[24:27]\n\t"
    "s_waitcnt lgkmcnt(1)\n\t"
    "v_cvt_pk_bf16_f32 v219, v198, v199\n\t"
    "v_mfma_f32_16x16x32_bf16 v[28:31], v[208:211], a[88:91], v[28:31]\n\t"
    "v_exp_f32 v201, v201\n\t"
    "v_mfma_f32_16x16x32_bf16 v[28:31], v[212:215], a[92:95], v[28:31]\n\t"
    "v_exp_f32 v206, v206\n\t"
    "v_mfma_f32_16x16x32_bf16 v[32:35], v[208:211], a[96:99], v[32:35]\n\t"
    "v_exp_f32 v204, v204\n\t"
    "v_mfma_f32_16x16x32_bf16 v[32:35], v[212:215], a[100:103], v[32:35]\n\t"
    "s_waitcnt lgkmcnt(0)\n\t"
    "v_cvt_pk_bf16_f32 v221, v202, v203\n\t"
    "v_mfma_f32_16x16x32_bf16 v[36:39], v[208:211], a[104:107], v[36:39]\n\t"
    "v_exp_f32 v195, v195\n\t"
    "v_mfma_f32_16x16x32_bf16 v[36:39], v[212:215], a[108:111], v[36:39]\n\t"
    "v_exp_f32 v200, v200\n\t"
    "v_mfma_f32_16x16x32_bf16 v[40:43], v[208:211], a[112:115], v[40:43]\n\t"
    "v_exp_f32 v207, v207\n\t"
    "v_mfma_f32_16x16x32_bf16 v[40:43], v[212:215], a[116:119], v[40:43]\n\t"
    "v_cvt_pk_bf16_f32 v216, v192, v193\n\t"
    "v_mfma_f32_16x16x32_bf16 v[44:47], v[208:211], a[120:123], v[44:47]\n\t"
    "v_exp_f32 v205, v205\n\t"
    "v_mfma_f32_16x16x32_bf16 v[44:47], v[212:215], a[124:127], v[44:47]\n\t"
    "v_cvt_pk_bf16_f32 v218, v196, v197\n\t"
    "v_smfmac_f32_16x16x64_bf16 a[212:215], a[208:211], v[208:215], v0\n\t"
    "v_cvt_pk_bf16_f32 v222, v204, v205\n\t"
    "s_waitcnt vmcnt(32)\n\t"
    "s_barrier\n\t"
    "v_mfma_f32_16x16x32_bf16 v[176:179], a[0:3], a[160:163], v[232:235]\n\t"
    "ds_read_b64_tr_b16 a[246:247], v3 offset:20480\n\t"
    "v_mfma_f32_16x16x32_bf16 v[176:179], a[4:7], a[164:167], v[176:179]\n\t"
    "ds_read_b64_tr_b16 a[252:253], v2 offset:16384\n\t"
    "s_add_u32 m0, s56, 16384\n\t"
    "v_mfma_f32_16x16x32_bf16 v[176:179], a[8:11], a[168:171], v[176:179]\n\t"
    "ds_read_b64_tr_b16 a[240:241], v3 offset:24576\n\t"
    "ds_read_b64_tr_b16 a[234:235], v2 offset:28928\n\t"
    "v_mfma_f32_16x16x32_bf16 v[176:179], a[12:15], a[172:175], v[176:179]\n\t"
    "ds_read_b128 v[252:255], v14 offset:9216\n\t"
    "buffer_load_dwordx4 v4, s[40:43], s57 offen nt lds\n\t"
    "v_mfma_f32_16x16x32_bf16 v[180:183], a[16:19], a[160:163], v[232:235]\n\t"
    "buffer_load_dwordx4 v5, s[40:43], s57 offen offset:1024 nt lds\n\t"
    "v_mfma_f32_16x16x32_bf16 v[180:183], a[20:23], a[164:167], v[180:183]\n\t"
    "ds_read_b64_tr_b16 a[254:255], v2 offset:20480\n\t"
    "ds_read_b128 v[244:247], v14 offset:1024\n\t"
    "v_mfma_f32_16x16x32_bf16 v[180:183], a[24:27], a[168:171], v[180:183]\n\t"
    "ds_read_b64_tr_b16 a[236:237], v2 offset:16640\n\t"
    "buffer_load_dwordx4 v6, s[40:43], s57 offen offset:2048 nt lds\n\t"
    "v_mfma_f32_16x16x32_bf16 v[180:183], a[28:31], a[172:175], v[180:183]\n\t"
    "ds_read_b128 v[240:243], v14 offset:0\n\t"
    "buffer_load_dwordx4 v7, s[40:43], s57 offen offset:3072 nt lds\n\t"
    "v_mfma_f32_16x16x32_bf16 v[184:187], a[32:35], a[160:163], v[232:235]\n\t"
    "v_cvt_pk_bf16_f32 v217, v194, v195\n\t"
    "v_cvt_pk_bf16_f32 v220, v200, v201\n\t"
    "v_mfma_f32_16x16x32_bf16 v[184:187], a[36:39], a[164:167], v[184:187]\n\t"
    "v_cvt_pk_bf16_f32 v223, v206, v207\n\t"
    "s_add_u32 m0, s56, 20480\n\t"
    "v_mfma_f32_16x16x32_bf16 v[184:187], a[40:43], a[168:171], v[184:187]\n\t"
    "ds_read_b64_tr_b16 a[244:245], v3 offset:16384\n\t"
    "ds_read_b64_tr_b16 a[238:239], v2 offset:20736\n\t"
    "v_mfma_f32_16x16x32_bf16 v[184:187], a[44:47], a[172:175], v[184:187]\n\t"
    "buffer_load_dwordx4 v4, s[40:43], s63 offen nt lds\n\t"
    "v_mfma_f32_16x16x32_bf16 v[188:191], a[48:51], a[160:163], v[232:235]\n\t"
    "v_exp_f32 v177, v177\n\t"
    "v_mfma_f32_16x16x32_bf16 v[188:191], a[52:55], a[164:167], v[188:191]\n\t"
    "buffer_load_dwordx4 v5, s[40:43], s63 offen offset:1024 nt lds\n\t"
    "v_mfma_f32_16x16x32_bf16 v[188:191], a[56:59], a[168:171], v[188:191]\n\t"
    "buffer_load_dwordx4 v6, s[40:43], s63 offen offset:2048 nt lds\n\t"
    "v_mfma_f32_16x16x32_bf16 v[188:191], a[60:63], a[172:175], v[188:191]\n\t"
    "ds_read_b64_tr_b16 a[250:251], v2 offset:28672\n\t"
    "ds_read_b64_tr_b16 a[242:243], v3 offset:28672\n\t"
    "v_mfma_f32_16x16x32_bf16 v[48:51], v[216:219], a[64:67], v[48:51]\n\t"
    "v_exp_f32 v183, v183\n\t"
    "v_mfma_f32_16x16x32_bf16 v[48:51], v[220:223], a[68:71], v[48:51]\n\t"
    "buffer_load_dwordx4 v7, s[40:43], s63 offen offset:3072 nt lds\n\t"
    "v_mfma_f32_16x16x32_bf16 v[52:55], v[216:219], a[72:75], v[52:55]\n\t"
    "ds_read_b64_tr_b16 a[232:233], v2 offset:24832\n\t"
    "s_add_u32 m0, s56, 24576\n\t"
    "v_mfma_f32_16x16x32_bf16 v[52:55], v[220:223], a[76:79], v[52:55]\n\t"
    "buffer_load_dwordx4 v4, s[40:43], s64 offen nt lds\n\t"
    "v_mfma_f32_16x16x32_bf16 v[56:59], v[216:219], a[80:83], v[56:59]\n\t"
    "buffer_load_dwordx4 v5, s[40:43], s64 offen offset:1024 nt lds\n\t"
    "v_mfma_f32_16x16x32_bf16 v[56:59], v[220:223], a[84:87], v[56:59]\n\t"
    "buffer_load_dwordx4 v6, s[40:43], s64 offen offset:2048 nt lds\n\t"
    "v_mfma_f32_16x16x32_bf16 v[60:63], v[216:219], a[88:91], v[60:63]\n\t"
    "v_exp_f32 v182, v182\n\t"
    "v_mfma_f32_16x16x32_bf16 v[60:63], v[220:223], a[92:95], v[60:63]\n\t"
    "v_exp_f32 v189, v189\n\t"
    "v_mfma_f32_16x16x32_bf16 v[64:67], v[216:219], a[96:99], v[64:67]\n\t"
    "buffer_load_dwordx4 v7, s[40:43], s64 offen offset:3072 nt lds\n\t"
    "v_mfma_f32_16x16x32_bf16 v[64:67], v[220:223], a[100:103], v[64:67]\n\t"
    "ds_read_b128 v[248:251], v14 offset:8192\n\t"
    "s_add_u32 m0, s56, 28672\n\t"
    "v_mfma_f32_16x16x32_bf16 v[68:71], v[216:219], a[104:107], v[68:71]\n\t"
    "buffer_load_dwordx4 v4, s[40:43], s65 offen nt lds\n\t"
    "v_mfma_f32_16x16x32_bf16 v[68:71], v[220:223], a[108:111], v[68:71]\n\t"
    "v_exp_f32 v187, v187\n\t"
    "v_mfma_f32_16x16x32_bf16 v[72:75], v[216:219], a[112:115], v[72:75]\n\t"
    "buffer_load_dwordx4 v5, s[40:43], s65 offen offset:1024 nt lds\n\t"
    "v_mfma_f32_16x16x32_bf16 v[72:75], v[220:223], a[116:119], v[72:75]\n\t"
    "v_exp_f32 v181, v181\n\t"
    "v_mfma_f32_16x16x32_bf16 v[76:79], v[216:219], a[120:123], v[76:79]\n\t"
    "buffer_load_dwordx4 v6, s[40:43], s65 offen offset:2048 nt lds\n\t"
    "v_mfma_f32_16x16x32_bf16 v[76:79], v[220:223], a[124:127], v[76:79]\n\t"
    "buffer_load_dwordx4 v7, s[40:43], s65 offen offset:3072 nt lds\n\t"
    "v_smfmac_f32_16x16x64_bf16 a[216:219], a[208:211], v[216:223], v0\n\t"
    "s_waitcnt lgkmcnt(6)\n\t"
    "v_mfma_f32_16x16x32_bf16 v[144:147], v[240:243], a[96:99], v[144:147]\n\t"
    "v_exp_f32 v186, v186\n\t"
    "v_mfma_f32_16x16x32_bf16 v[144:147], v[244:247], a[100:103], v[144:147]\n\t"
    "v_exp_f32 v180, v180\n\t"
    "v_mfma_f32_16x16x32_bf16 v[148:151], v[240:243], a[104:107], v[148:151]\n\t"
    "ds_read_b64_tr_b16 a[248:249], v2 offset:24576\n\t"
    "s_add_u32 m0, s56, 81920\n\t"
    "v_mfma_f32_16x16x32_bf16 v[148:151], v[244:247], a[108:111], v[148:151]\n\t"
    "buffer_load_dwordx4 v4, s[44:47], s57 offen nt lds\n\t"
    "v_mfma_f32_16x16x32_bf16 v[152:155], v[240:243], a[112:115], v[152:155]\n\t"
    "v_exp_f32 v184, v184\n\t"
    "v_mfma_f32_16x16x32_bf16 v[152:155], v[244:247], a[116:119], v[152:155]\n\t"
    "buffer_load_dwordx4 v5, s[44:47], s57 offen offset:1024 nt lds\n\t"
    "v_mfma_f32_16x16x32_bf16 v[156:159], v[240:243], a[120:123], v[156:159]\n\t"
    "v_exp_f32 v178, v178\n\t"
    "v_mfma_f32_16x16x32_bf16 v[156:159], v[244:247], a[124:127], v[156:159]\n\t"
    "s_waitcnt lgkmcnt(1)\n\t"
    "buffer_load_dwordx4 v4, s[44:47], s57 offen offset:2048 nt lds\n\t"
    "v_mfma_f32_16x16x32_bf16 v[160:163], v[248:251], a[96:99], v[160:163]\n\t"
    "buffer_load_dwordx4 v5, s[44:47], s57 offen offset:3072 nt lds\n\t"
    "v_mfma_f32_16x16x32_bf16 v[160:163], v[252:255], a[100:103], v[160:163]\n\t"
    "v_cvt_pk_bf16_f32 v210, v180, v181\n\t"
    "s_add_u32 m0, s56, 86016\n\t"
    "v_mfma_f32_16x16x32_bf16 v[164:167], v[248:251], a[104:107], v[164:167]\n\t"
    "buffer_load_dwordx4 v4, s[44:47], s63 offen nt lds\n\t"
    "v_mfma_f32_16x16x32_bf16 v[164:167], v[252:255], a[108:111], v[164:167]\n\t"
    "v_exp_f32 v188, v188\n\t"
    "v_mfma_f32_16x16x32_bf16 v[192:195], a[0:3], a[176:179], v[236:239]\n\t"
    "buffer_load_dwordx4 v5, s[44:47], s63 offen offset:1024 nt lds\n\t"
    "v_mfma_f32_16x16x32_bf16 v[192:195], a[4:7], a[180:183], v[192:195]\n\t"
    "buffer_load_dwordx4 v4, s[44:47], s63 offen offset:2048 nt lds\n\t"
    "v_mfma_f32_16x16x32_bf16 v[192:195], a[8:11], a[184:187], v[192:195]\n\t"
    "v_exp_f32 v185, v185\n\t"
    "v_mfma_f32_16x16x32_bf16 v[192:195], a[12:15], a[188:191], v[192:195]\n\t"
    "v_exp_f32 v191, v191\n\t"
    "v_mfma_f32_16x16x32_bf16 v[196:199], a[16:19], a[176:179], v[236:239]\n\t"
    "v_exp_f32 v176, v176\n\t"
    "v_mfma_f32_16x16x32_bf16 v[196:199], a[20:23], a[180:183], v[196:199]\n\t"
    "v_cvt_pk_bf16_f32 v211, v182, v183\n\t"
    "v_cvt_pk_bf16_f32 v214, v188, v189\n\t"
    "v_mfma_f32_16x16x32_bf16 v[196:199], a[24:27], a[184:187], v[196:199]\n\t"
    "buffer_load_dwordx4 v5, s[44:47], s63 offen offset:3072 nt lds\n\t"
    "v_mfma_f32_16x16x32_bf16 v[196:199], a[28:31], a[188:191], v[196:199]\n\t"
    "v_cvt_pk_bf16_f32 v213, v186, v187\n\t"
    "s_add_u32 m0, s56, 90112\n\t"
    "v_mfma_f32_16x16x32_bf16 v[200:203], a[32:35], a[176:179], v[236:239]\n\t"
    "buffer_load_dwordx4 v4, s[44:47], s64 offen nt lds\n\t"
    "v_mfma_f32_16x16x32_bf16 v[200:203], a[36:39], a[180:183], v[200:203]\n\t"
    "buffer_load_dwordx4 v5, s[44:47], s64 offen offset:1024 nt lds\n\t"
    "v_mfma_f32_16x16x32_bf16 v[200:203], a[40:43], a[184:187], v[200:203]\n\t"
    "v_exp_f32 v179, v179\n\t"
    "v_mfma_f32_16x16x32_bf16 v[200:203], a[44:47], a[188:191], v[200:203]\n\t"
    "v_exp_f32 v190, v190\n\t"
    "v_mfma_f32_16x16x32_bf16 v[204:207], a[48:51], a[176:179], v[236:239]\n\t"
    "v_cvt_pk_bf16_f32 v208, v176, v177\n\t"
    "v_cvt_pk_bf16_f32 v209, v178, v179\n\t"
    "v_mfma_f32_16x16x32_bf16 v[204:207], a[52:55], a[180:183], v[204:207]\n\t"
    "buffer_load_dwordx4 v4, s[44:47], s64 offen offset:2048 nt lds\n\t"
    "v_mfma_f32_16x16x32_bf16 v[204:207], a[56:59], a[184:187], v[204:207]\n\t"
    "v_cvt_pk_bf16_f32 v212, v184, v185\n\t"
    "v_cvt_pk_bf16_f32 v215, v190, v191\n\t"
    "v_mfma_f32_16x16x32_bf16 v[204:207], a[60:63], a[188:191], v[204:207]\n\t"
    "buffer_load_dwordx4 v5, s[44:47], s64 offen offset:3072 nt lds\n\t"
    "v_mfma_f32_16x16x32_bf16 v[80:83], v[208:211], a[64:67], v[80:83]\n\t"
    "ds_read_b128 a[8:11], v1 offset:16896\n\t"
    "s_add_u32 m0, s56, 94208\n\t"
    "v_mfma_f32_16x16x32_bf16 v[80:83], v[212:215], a[68:71], v[80:83]\n\t"
    "buffer_load_dwordx4 v4, s[44:47], s65 offen nt lds\n\t"
    "v_mfma_f32_16x16x32_bf16 v[84:87], v[208:211], a[72:75], v[84:87]\n\t"
    "buffer_load_dwordx4 v5, s[44:47], s65 offen offset:1024 nt lds\n\t"
    "v_mfma_f32_16x16x32_bf16 v[84:87], v[212:215], a[76:79], v[84:87]\n\t"
    "ds_read_b128 a[24:27], v1 offset:20992\n\t"
    "ds_read_b128 a[32:35], v1 offset:24576\n\t"
    "v_mfma_f32_16x16x32_bf16 v[88:91], v[208:211], a[80:83], v[88:91]\n\t"
    "buffer_load_dwordx4 v4, s[44:47], s65 offen offset:2048 nt lds\n\t"
    "v_mfma_f32_16x16x32_bf16 v[88:91], v[212:215], a[84:87], v[88:91]\n\t"
    "ds_read_b128 a[16:19], v1 offset:20480\n\t"
    "ds_read_b128 a[40:43], v1 offset:25088\n\t"
    "v_mfma_f32_16x16x32_bf16 v[92:95], v[208:211], a[88:91], v[92:95]\n\t"
    "v_exp_f32 v196, v196\n\t"
    "v_mfma_f32_16x16x32_bf16 v[92:95], v[212:215], a[92:95], v[92:95]\n\t"
    "ds_read_b128 a[4:7], v1 offset:16640\n\t"
    "ds_read_b128 a[36:39], v1 offset:24832\n\t"
    "v_mfma_f32_16x16x32_bf16 v[96:99], v[208:211], a[96:99], v[96:99]\n\t"
    "ds_read_b128 a[20:23], v1 offset:20736\n\t"
    "ds_read_b128 v[240:243], v14 offset:16384\n\t"
    "v_mfma_f32_16x16x32_bf16 v[96:99], v[212:215], a[100:103], v[96:99]\n\t"
    "v_exp_f32 v205, v205\n\t"
    "v_mfma_f32_16x16x32_bf16 v[100:103], v[208:211], a[104:107], v[100:103]\n\t"
    "v_exp_f32 v194, v194\n\t"
    "v_mfma_f32_16x16x32_bf16 v[100:103], v[212:215], a[108:111], v[100:103]\n\t"
    "ds_read_b128 a[12:15], v1 offset:17152\n\t"
    "s_waitcnt vmcnt(62)\n\t"
    "v_mfma_f32_16x16x32_bf16 v[104:107], v[208:211], a[112:115], v[104:107]\n\t"
    "ds_read_b128 a[0:3], v1 offset:16384\n\t"
    "ds_read_b128 v[244:247], v14 offset:17408\n\t"
    "v_mfma_f32_16x16x32_bf16 v[104:107], v[212:215], a[116:119], v[104:107]\n\t"
    "buffer_load_dwordx4 v5, s[44:47], s65 offen offset:3072 nt lds\n\t"
    "v_mfma_f32_16x16x32_bf16 v[108:111], v[208:211], a[120:123], v[108:111]\n\t"
    "v_exp_f32 v199, v199\n\t"
    "v_mfma_f32_16x16x32_bf16 v[108:111], v[212:215], a[124:127], v[108:111]\n\t"
    "ds_read_b128 a[28:31], v1 offset:21248\n\t"
    "ds_read_b128 a[56:59], v1 offset:29184\n\t"
    "v_smfmac_f32_16x16x64_bf16 a[220:223], a[208:211], v[208:215], v0\n\t"
    "v_mfma_f32_16x16x32_bf16 v[168:171], v[248:251], a[112:115], v[168:171]\n\t"
    "ds_read_b128 a[60:63], v1 offset:29440\n\t"
    "v_mfma_f32_16x16x32_bf16 v[168:171], v[252:255], a[116:119], v[168:171]\n\t"
    "s_waitcnt lgkmcnt(3)\n\t"
    "s_add_u32 s57, s57, 16384\n\t"
    "v_mfma_f32_16x16x32_bf16 v[172:175], v[248:251], a[120:123], v[172:175]\n\t"
    "ds_read_b128 a[44:47], v1 offset:25344\n\t"
    "s_add_u32 s63, s63, 16384\n\t"
    "v_mfma_f32_16x16x32_bf16 v[172:175], v[252:255], a[124:127], v[172:175]\n\t"
    "v_exp_f32 v203, v203\n\t"
    "v_mfma_f32_16x16x32_bf16 a[192:195], v[240:243], a[96:99], a[192:195]\n\t"
    "ds_read_b128 a[48:51], v1 offset:28672\n\t"
    "s_add_u32 s64, s64, 16384\n\t"
    "v_mfma_f32_16x16x32_bf16 a[192:195], v[244:247], a[100:103], a[192:195]\n\t"
    "v_exp_f32 v206, v206\n\t"
    "v_mfma_f32_16x16x32_bf16 a[196:199], v[240:243], a[104:107], a[196:199]\n\t"
    "v_exp_f32 v193, v193\n\t"
    "v_mfma_f32_16x16x32_bf16 a[196:199], v[244:247], a[108:111], a[196:199]\n\t"
    "ds_read_b128 a[52:55], v1 offset:28928\n\t"
    "s_add_u32 s65, s65, 16384\n\t"
    "v_mfma_f32_16x16x32_bf16 a[200:203], v[240:243], a[112:115], a[200:203]\n\t"
    "v_exp_f32 v198, v198\n\t"
    "v_mfma_f32_16x16x32_bf16 a[200:203], v[244:247], a[116:119], a[200:203]\n\t"
    "v_exp_f32 v207, v207\n\t"
    "v_mfma_f32_16x16x32_bf16 a[204:207], v[240:243], a[120:123], a[204:207]\n\t"
    "v_exp_f32 v201, v201\n\t"
    "v_mfma_f32_16x16x32_bf16 a[204:207], v[244:247], a[124:127], a[204:207]\n\t"
    "v_cvt_pk_bf16_f32 v223, v206, v207\n\t"
    "s_waitcnt lgkmcnt(5)\n\t"
    "s_sub_u32 s62, s62, 1\n\t"
    "s_cbranch_scc0 LOOP4_%=\n\t"
    "v_mfma_f32_16x16x32_bf16 v[176:179], a[0:3], a[128:131], v[224:227]\n\t"
    "v_exp_f32 v197, v197\n\t"
    "v_mfma_f32_16x16x32_bf16 v[176:179], a[4:7], a[132:135], v[176:179]\n\t"
    "v_exp_f32 v192, v192\n\t"
    "ds_write_b128 v14, v[144:147] offset:0\n\t"
    "v_mfma_f32_16x16x32_bf16 v[176:179], a[8:11], a[136:139], v[176:179]\n\t"
    "v_cvt_pk_bf16_f32 v216, v192, v193\n\t"
    "v_cvt_pk_bf16_f32 v218, v196, v197\n\t"
    "ds_write_b128 v14, v[148:151] offset:1024\n\t"
    "v_mfma_f32_16x16x32_bf16 v[176:179], a[12:15], a[140:143], v[176:179]\n\t"
    "v_exp_f32 v204, v204\n\t"
    "ds_write_b128 v14, v[152:155] offset:2048\n\t"
    "v_mfma_f32_16x16x32_bf16 v[180:183], a[16:19], a[128:131], v[224:227]\n\t"
    "v_exp_f32 v200, v200\n\t"
    "ds_write_b128 v14, v[156:159] offset:3072\n\t"
    "v_mfma_f32_16x16x32_bf16 v[180:183], a[20:23], a[132:135], v[180:183]\n\t"
    "s_waitcnt lgkmcnt(4)\n\t"
    "ds_write_b128 v14, v[160:163] offset:8192\n\t"
    "v_mfma_f32_16x16x32_bf16 v[180:183], a[24:27], a[136:139], v[180:183]\n\t"
    "v_exp_f32 v202, v202\n\t"
    "ds_write_b128 v14, v[164:167] offset:9216\n\t"
    "v_mfma_f32_16x16x32_bf16 v[180:183], a[28:31], a[140:143], v[180:183]\n\t"
    "v_exp_f32 v195, v195\n\t"
    "ds_write_b128 v14, v[168:171] offset:10240\n\t"
    "v_mfma_f32_16x16x32_bf16 v[184:187], a[32:35], a[128:131], v[224:227]\n\t"
    "v_cvt_pk_bf16_f32 v217, v194, v195\n\t"
    "v_cvt_pk_bf16_f32 v222, v204, v205\n\t"
    "ds_write_b128 v14, v[172:175] offset:11264\n\t"
    "v_mfma_f32_16x16x32_bf16 v[184:187], a[36:39], a[132:135], v[184:187]\n\t"
    "v_cvt_pk_bf16_f32 v219, v198, v199\n\t"
    "v_cvt_pk_bf16_f32 v220, v200, v201\n\t"
    "ds_write_b128 v14, a[192:195] offset:16384\n\t"
    "v_mfma_f32_16x16x32_bf16 v[184:187], a[40:43], a[136:139], v[184:187]\n\t"
    "v_exp_f32 v176, v176\n\t"
    "ds_write_b128 v14, a[196:199] offset:17408\n\t"
    "v_mfma_f32_16x16x32_bf16 v[184:187], a[44:47], a[140:143], v[184:187]\n\t"
    "v_exp_f32 v177, v177\n\t"
    "ds_write_b128 v14, a[200:203] offset:18432\n\t"
    "v_mfma_f32_16x16x32_bf16 v[188:191], a[48:51], a[128:131], v[224:227]\n\t"
    "v_cvt_pk_bf16_f32 v221, v202, v203\n\t"
    "v_cvt_pk_bf16_f32 v208, v176, v177\n\t"
    "ds_write_b128 v14, a[204:207] offset:19456\n\t"
    "v_mfma_f32_16x16x32_bf16 v[188:191], a[52:55], a[132:135], v[188:191]\n\t"
    "v_exp_f32 v179, v179\n\t"
    "v_mfma_f32_16x16x32_bf16 v[188:191], a[56:59], a[136:139], v[188:191]\n\t"
    "v_exp_f32 v182, v182\n\t"
    "v_mfma_f32_16x16x32_bf16 v[188:191], a[60:63], a[140:143], v[188:191]\n\t"
    "v_exp_f32 v178, v178\n\t"
    "v_mfma_f32_16x16x32_bf16 v[124:127], v[216:219], a[88:91], v[124:127]\n\t"
    "ds_read_b64_tr_b16 a[90:91], v3 offset:20736\n\t"
    "v_mfma_f32_16x16x32_bf16 v[124:127], v[220:223], a[92:95], v[124:127]\n\t"
    "v_exp_f32 v181, v181\n\t"
    "v_mfma_f32_16x16x32_bf16 v[128:131], v[216:219], a[96:99], v[128:131]\n\t"
    "ds_read_b64_tr_b16 a[88:89], v3 offset:16640\n\t"
    "ds_read_b64_tr_b16 a[94:95], v3 offset:28928\n\t"
    "v_mfma_f32_16x16x32_bf16 v[128:131], v[220:223], a[100:103], v[128:131]\n\t"
    "ds_read_b64_tr_b16 a[100:101], v2 offset:25088\n\t"
    "s_waitcnt lgkmcnt(4)\n\t"
    "s_barrier\n\t"
    "v_mfma_f32_16x16x32_bf16 v[132:135], v[216:219], a[104:107], v[132:135]\n\t"
    "ds_read_b64_tr_b16 a[98:99], v2 offset:20992\n\t"
    "v_mfma_f32_16x16x32_bf16 v[132:135], v[220:223], a[108:111], v[132:135]\n\t"
    "ds_read_b64_tr_b16 a[92:93], v3 offset:24832\n\t"
    "ds_read_b64_tr_b16 a[106:107], v3 offset:20992\n\t"
    "v_mfma_f32_16x16x32_bf16 v[136:139], v[216:219], a[112:115], v[136:139]\n\t"
    "v_exp_f32 v180, v180\n\t"
    "v_mfma_f32_16x16x32_bf16 v[136:139], v[220:223], a[116:119], v[136:139]\n\t"
    "ds_read_b64_tr_b16 a[110:111], v3 offset:29184\n\t"
    "ds_read_b64_tr_b16 a[118:119], v2 offset:29440\n\t"
    "v_mfma_f32_16x16x32_bf16 v[140:143], v[216:219], a[120:123], v[140:143]\n\t"
    "ds_read_b64_tr_b16 a[96:97], v2 offset:16896\n\t"
    "ds_read_b64_tr_b16 a[102:103], v2 offset:29184\n\t"
    "v_mfma_f32_16x16x32_bf16 v[140:143], v[220:223], a[124:127], v[140:143]\n\t"
    "ds_read_b64_tr_b16 a[114:115], v2 offset:21248\n\t"
    "v_mfma_f32_16x16x32_bf16 v[112:115], v[216:219], a[64:67], v[112:115]\n\t"
    "ds_read_b64_tr_b16 a[108:109], v3 offset:25088\n\t"
    "ds_read_b64_tr_b16 a[120:121], v3 offset:17152\n\t"
    "v_mfma_f32_16x16x32_bf16 v[112:115], v[220:223], a[68:71], v[112:115]\n\t"
    "v_exp_f32 v185, v185\n\t"
    "v_mfma_f32_16x16x32_bf16 v[116:119], v[216:219], a[72:75], v[116:119]\n\t"
    "ds_read_b64_tr_b16 a[112:113], v2 offset:17152\n\t"
    "ds_read_b64_tr_b16 a[116:117], v2 offset:25344\n\t"
    "v_mfma_f32_16x16x32_bf16 v[116:119], v[220:223], a[76:79], v[116:119]\n\t"
    "ds_read_b64_tr_b16 a[124:125], v3 offset:25344\n\t"
    "ds_read_b64_tr_b16 a[126:127], v3 offset:29440\n\t"
    "v_mfma_f32_16x16x32_bf16 v[120:123], v[216:219], a[80:83], v[120:123]\n\t"
    "v_exp_f32 v188, v188\n\t"
    "v_mfma_f32_16x16x32_bf16 v[120:123], v[220:223], a[84:87], v[120:123]\n\t"
    "ds_read_b64_tr_b16 a[104:105], v3 offset:16896\n\t"
    "ds_read_b64_tr_b16 a[122:123], v3 offset:21248\n\t"
    "v_smfmac_f32_16x16x64_bf16 a[224:227], a[208:211], v[216:223], v0\n\t"
    "v_mfma_f32_16x16x32_bf16 v[192:195], a[0:3], a[144:147], v[228:231]\n\t"
    "v_exp_f32 v190, v190\n\t"
    "v_mbcnt_lo_u32_b32 v11, -1, 0\n\t"
    "v_mbcnt_hi_u32_b32 v11, -1, v11\n\t"
    "v_and_b32 v9, 15, v11\n\t"
    "v_mfma_f32_16x16x32_bf16 v[192:195], a[4:7], a[148:151], v[192:195]\n\t"
    "v_exp_f32 v191, v191\n\t"
    "v_lshlrev_b32 v8, 8, v9\n\t"
    "v_bfe_u32 v10, v11, 4, 1\n\t"
    "v_lshl_add_u32 v8, v10, 5, v8\n\t"
    "v_mfma_f32_16x16x32_bf16 v[192:195], a[8:11], a[152:155], v[192:195]\n\t"
    "v_exp_f32 v187, v187\n\t"
    "v_lshrrev_b32 v10, 5, v11\n\t"
    "v_lshl_add_u32 v8, v10, 4, v8\n\t"
    "v_mfma_f32_16x16x32_bf16 v[192:195], a[12:15], a[156:159], v[192:195]\n\t"
    "v_exp_f32 v184, v184\n\t"
    "s_mul_i32 s58, %[wid], 20480\n\t"
    "s_waitcnt vmcnt(0)\n\t"
    "s_mov_b64 s[60:61], 0\n\t"
    "v_mfma_f32_16x16x32_bf16 v[196:199], a[16:19], a[144:147], v[228:231]\n\t"
    "v_cvt_pk_bf16_f32 v209, v178, v179\n\t"
    "v_cvt_pk_bf16_f32 v212, v184, v185\n\t"
    "s_mov_b32 s59, 0x53800000\n\t"
    "v_mov_b32 v4, 0\n\t"
    "v_mov_b32 v5, 0\n\t"
    "v_mfma_f32_16x16x32_bf16 v[196:199], a[20:23], a[148:151], v[196:199]\n\t"
    "v_exp_f32 v189, v189\n\t"
    "v_lshrrev_b32 v10, 4, v11\n\t"
    "v_lshlrev_b32 v12, 2, v10\n\t"
    "v_mfma_f32_16x16x32_bf16 v[196:199], a[24:27], a[152:155], v[196:199]\n\t"
    "v_exp_f32 v186, v186\n\t"
    "v_xor_b32 v12, v12, v9\n\t"
    "v_lshlrev_b32 v12, 4, v12\n\t"
    "v_lshl_add_u32 v224, v10, 8, v12\n\t"
    "v_mfma_f32_16x16x32_bf16 v[196:199], a[28:31], a[156:159], v[196:199]\n\t"
    "v_cvt_pk_bf16_f32 v214, v188, v189\n\t"
    "v_cvt_pk_bf16_f32 v215, v190, v191\n\t"
    "v_add_u32 v224, s67, v224\n\t"
    "v_add_u32 v224, 0x8000, v224\n\t"
    "v_mfma_f32_16x16x32_bf16 v[200:203], a[32:35], a[144:147], v[228:231]\n\t"
    "v_exp_f32 v183, v183\n\t"
    "v_and_b32 v12, 3, v11\n\t"
    "v_bfe_u32 v13, v11, 2, 2\n\t"
    "v_lshrrev_b32 v10, 5, v11\n\t"
    "v_mfma_f32_16x16x32_bf16 v[200:203], a[36:39], a[148:151], v[200:203]\n\t"
    "v_cvt_pk_bf16_f32 v211, v182, v183\n\t"
    "v_lshl_add_u32 v13, v10, 3, v13\n\t"
    "v_lshlrev_b32 v10, 2, v12\n\t"
    "v_xor_b32 v13, v13, v10\n\t"
    "v_mfma_f32_16x16x32_bf16 v[200:203], a[40:43], a[152:155], v[200:203]\n\t"
    "v_exp_f32 v194, v194\n\t"
    "v_lshlrev_b32 v13, 4, v13\n\t"
    "v_lshl_add_u32 v13, v12, 8, v13\n\t"
    "v_mfma_f32_16x16x32_bf16 v[200:203], a[44:47], a[156:159], v[200:203]\n\t"
    "v_exp_f32 v192, v192\n\t"
    "v_bfe_u32 v10, v11, 4, 1\n\t"
    "v_lshl_add_u32 v13, v10, 3, v13\n\t"
    "v_add_u32 v13, s67, v13\n\t"
    "v_mfma_f32_16x16x32_bf16 v[204:207], a[48:51], a[144:147], v[228:231]\n\t"
    "v_cvt_pk_bf16_f32 v210, v180, v181\n\t"
    "v_cvt_pk_bf16_f32 v213, v186, v187\n\t"
    "v_add_u32 v225, 0x8000, v13\n\t"
    "v_xor_b32 v226, 64, v225\n\t"
    "v_mfma_f32_16x16x32_bf16 v[204:207], a[52:55], a[148:151], v[204:207]\n\t"
    "s_waitcnt lgkmcnt(15)\n\t"
    "v_mfma_f32_16x16x32_bf16 v[204:207], a[56:59], a[152:155], v[204:207]\n\t"
    "v_exp_f32 v198, v198\n\t"
    "v_mfma_f32_16x16x32_bf16 v[204:207], a[60:63], a[156:159], v[204:207]\n\t"
    "v_exp_f32 v196, v196\n\t"
    "v_mfma_f32_16x16x32_bf16 v[16:19], v[208:211], a[252:255], v[16:19]\n\t"
    "v_exp_f32 v197, v197\n\t"
    "v_mfma_f32_16x16x32_bf16 v[16:19], v[212:215], a[248:251], v[16:19]\n\t"
    "v_exp_f32 v199, v199\n\t"
    "v_mfma_f32_16x16x32_bf16 v[20:23], v[208:211], a[244:247], v[20:23]\n\t"
    "v_exp_f32 v193, v193\n\t"
    "v_mfma_f32_16x16x32_bf16 v[20:23], v[212:215], a[240:243], v[20:23]\n\t"
    "v_exp_f32 v203, v203\n\t"
    "v_mfma_f32_16x16x32_bf16 v[24:27], v[208:211], a[236:239], v[24:27]\n\t"
    "v_exp_f32 v202, v202\n\t"
    "v_mfma_f32_16x16x32_bf16 v[24:27], v[212:215], a[232:235], v[24:27]\n\t"
    "s_waitcnt lgkmcnt(1)\n\t"
    "v_cvt_pk_bf16_f32 v219, v198, v199\n\t"
    "v_mfma_f32_16x16x32_bf16 v[28:31], v[208:211], a[88:91], v[28:31]\n\t"
    "v_exp_f32 v201, v201\n\t"
    "v_mfma_f32_16x16x32_bf16 v[28:31], v[212:215], a[92:95], v[28:31]\n\t"
    "v_exp_f32 v206, v206\n\t"
    "v_mfma_f32_16x16x32_bf16 v[32:35], v[208:211], a[96:99], v[32:35]\n\t"
    "v_exp_f32 v204, v204\n\t"
    "v_mfma_f32_16x16x32_bf16 v[32:35], v[212:215], a[100:103], v[32:35]\n\t"
    "s_waitcnt lgkmcnt(0)\n\t"
    "v_cvt_pk_bf16_f32 v221, v202, v203\n\t"
    "v_mfma_f32_16x16x32_bf16 v[36:39], v[208:211], a[104:107], v[36:39]\n\t"
    "v_exp_f32 v195, v195\n\t"
    "v_mfma_f32_16x16x32_bf16 v[36:39], v[212:215], a[108:111], v[36:39]\n\t"
    "v_exp_f32 v200, v200\n\t"
    "v_mfma_f32_16x16x32_bf16 v[40:43], v[208:211], a[112:115], v[40:43]\n\t"
    "v_exp_f32 v207, v207\n\t"
    "v_mfma_f32_16x16x32_bf16 v[40:43], v[212:215], a[116:119], v[40:43]\n\t"
    "v_cvt_pk_bf16_f32 v216, v192, v193\n\t"
    "v_mfma_f32_16x16x32_bf16 v[44:47], v[208:211], a[120:123], v[44:47]\n\t"
    "v_exp_f32 v205, v205\n\t"
    "v_mfma_f32_16x16x32_bf16 v[44:47], v[212:215], a[124:127], v[44:47]\n\t"
    "v_cvt_pk_bf16_f32 v218, v196, v197\n\t"
    "v_smfmac_f32_16x16x64_bf16 a[212:215], a[208:211], v[208:215], v0\n\t"
    "v_cvt_pk_bf16_f32 v222, v204, v205\n\t"
    "v_mfma_f32_16x16x32_bf16 v[176:179], a[0:3], a[160:163], v[232:235]\n\t"
    "v_mfma_f32_16x16x32_bf16 v[176:179], a[4:7], a[164:167], v[176:179]\n\t"
    "v_mfma_f32_16x16x32_bf16 v[176:179], a[8:11], a[168:171], v[176:179]\n\t"
    "v_mfma_f32_16x16x32_bf16 v[176:179], a[12:15], a[172:175], v[176:179]\n\t"
    "v_mfma_f32_16x16x32_bf16 v[180:183], a[16:19], a[160:163], v[232:235]\n\t"
    "v_mfma_f32_16x16x32_bf16 v[180:183], a[20:23], a[164:167], v[180:183]\n\t"
    "s_nop 0\n\t"
    "v_accvgpr_read_b32 v10, a212\n\t"
    "v_accvgpr_read_b32 v11, a213\n\t"
    "v_add_f32 v10, v10, v11\n\t"
    "v_mfma_f32_16x16x32_bf16 v[180:183], a[24:27], a[168:171], v[180:183]\n\t"
    "v_cmp_nge_f32 vcc, s59, v10\n\t"
    "v_rcp_f32 v10, v10\n\t"
    "s_or_b64 s[60:61], s[60:61], vcc\n\t"
    "v_mfma_f32_16x16x32_bf16 v[180:183], a[28:31], a[172:175], v[180:183]\n\t"
    "v_mov_b32 v11, v10\n\t"
    "v_mov_b32_dpp v11, v10 row_shl:4 row_mask:0x2 bank_mask:0xf\n\t"
    "v_mov_b32_dpp v11, v10 row_shl:8 row_mask:0x4 bank_mask:0xf\n\t"
    "v_mfma_f32_16x16x32_bf16 v[184:187], a[32:35], a[160:163], v[232:235]\n\t"
    "v_cvt_pk_bf16_f32 v217, v194, v195\n\t"
    "v_cvt_pk_bf16_f32 v220, v200, v201\n\t"
    "v_mov_b32_dpp v11, v10 row_shl:12 row_mask:0x8 bank_mask:0xf\n\t"
    "s_nop 1\n\t"
    "v_mul_f32_dpp v16, v11, v16 row_newbcast:0 row_mask:0xf bank_mask:0xf\n\t"
    "v_mul_f32_dpp v17, v11, v17 row_newbcast:1 row_mask:0xf bank_mask:0xf\n\t"
    "v_mfma_f32_16x16x32_bf16 v[184:187], a[36:39], a[164:167], v[184:187]\n\t"
    "v_cvt_pk_bf16_f32 v223, v206, v207\n\t"
    "v_mul_f32_dpp v18, v11, v18 row_newbcast:2 row_mask:0xf bank_mask:0xf\n\t"
    "v_mul_f32_dpp v19, v11, v19 row_newbcast:3 row_mask:0xf bank_mask:0xf\n\t"
    "v_mfma_f32_16x16x32_bf16 v[184:187], a[40:43], a[168:171], v[184:187]\n\t"
    "v_mul_f32_dpp v20, v11, v20 row_newbcast:0 row_mask:0xf bank_mask:0xf\n\t"
    "v_mul_f32_dpp v21, v11, v21 row_newbcast:1 row_mask:0xf bank_mask:0xf\n\t"
    "v_mul_f32_dpp v22, v11, v22 row_newbcast:2 row_mask:0xf bank_mask:0xf\n\t"
    "v_mfma_f32_16x16x32_bf16 v[184:187], a[44:47], a[172:175], v[184:187]\n\t"
    "v_mul_f32_dpp v23, v11, v23 row_newbcast:3 row_mask:0xf bank_mask:0xf\n\t"
    "v_cvt_pk_bf16_f32 v23, v22, v23\n\t"
    "v_cvt_pk_bf16_f32 v22, v20, v21\n\t"
    "v_mfma_f32_16x16x32_bf16 v[188:191], a[48:51], a[160:163], v[232:235]\n\t"
    "v_exp_f32 v177, v177\n\t"
    "v_cvt_pk_bf16_f32 v21, v18, v19\n\t"
    "v_cvt_pk_bf16_f32 v20, v16, v17\n\t"
    "ds_write_b128 v224, v[20:23] offset:0\n\t"
    "v_mfma_f32_16x16x32_bf16 v[188:191], a[52:55], a[164:167], v[188:191]\n\t"
    "ds_read_b64_tr_b16 v[16:17], v225 offset:0\n\t"
    "ds_read_b64_tr_b16 v[18:19], v226 offset:0\n\t"
    "v_mul_f32_dpp v24, v11, v24 row_newbcast:0 row_mask:0xf bank_mask:0xf\n\t"
    "v_mfma_f32_16x16x32_bf16 v[188:191], a[56:59], a[168:171], v[188:191]\n\t"
    "v_mul_f32_dpp v25, v11, v25 row_newbcast:1 row_mask:0xf bank_mask:0xf\n\t"
    "v_mul_f32_dpp v26, v11, v26 row_newbcast:2 row_mask:0xf bank_mask:0xf\n\t"
    "v_mfma_f32_16x16x32_bf16 v[188:191], a[60:63], a[172:175], v[188:191]\n\t"
    "v_mul_f32_dpp v27, v11, v27 row_newbcast:3 row_mask:0xf bank_mask:0xf\n\t"
    "v_mul_f32_dpp v28, v11, v28 row_newbcast:0 row_mask:0xf bank_mask:0xf\n\t"
    "v_mul_f32_dpp v29, v11, v29 row_newbcast:1 row_mask:0xf bank_mask:0xf\n\t"
    "v_mfma_f32_16x16x32_bf16 v[48:51], v[216:219], a[252:255], v[48:51]\n\t"
    "v_exp_f32 v183, v183\n\t"
    "v_mul_f32_dpp v30, v11, v30 row_newbcast:2 row_mask:0xf bank_mask:0xf\n\t"
    "v_mul_f32_dpp v31, v11, v31 row_newbcast:3 row_mask:0xf bank_mask:0xf\n\t"
    "v_cvt_pk_bf16_f32 v24, v24, v25\n\t"
    "v_mfma_f32_16x16x32_bf16 v[48:51], v[220:223], a[248:251], v[48:51]\n\t"
    "v_cvt_pk_bf16_f32 v25, v26, v27\n\t"
    "v_cvt_pk_bf16_f32 v26, v28, v29\n\t"
    "v_cvt_pk_bf16_f32 v27, v30, v31\n\t"
    "v_mfma_f32_16x16x32_bf16 v[52:55], v[216:219], a[244:247], v[52:55]\n\t"
    "ds_write_b128 v224, v[24:27] offset:1024\n\t"
    "ds_read_b64_tr_b16 v[28:29], v225 offset:1024\n\t"
    "v_mfma_f32_16x16x32_bf16 v[52:55], v[220:223], a[240:243], v[52:55]\n\t"
    "ds_read_b64_tr_b16 v[30:31], v226 offset:1024\n\t"
    "v_smfmac_f32_16x16x64_bf16 v[4:7], a[208:211], v[20:27], v0\n\t"
    "s_waitcnt lgkmcnt(3)\n\t"
    "v_mfma_f32_16x16x32_bf16 v[56:59], v[216:219], a[236:239], v[56:59]\n\t"
    "buffer_store_dwordx4 v[16:19], v8, s[52:55], s58 offen sc0 sc1\n\t"
    "v_mul_f32_dpp v32, v11, v32 row_newbcast:0 row_mask:0xf bank_mask:0xf\n\t"
    "v_mul_f32_dpp v33, v11, v33 row_newbcast:1 row_mask:0xf bank_mask:0xf\n\t"
    "v_mfma_f32_16x16x32_bf16 v[56:59], v[220:223], a[232:235], v[56:59]\n\t"
    "v_mul_f32_dpp v34, v11, v34 row_newbcast:2 row_mask:0xf bank_mask:0xf\n\t"
    "v_mul_f32_dpp v35, v11, v35 row_newbcast:3 row_mask:0xf bank_mask:0xf\n\t"
    "v_mul_f32_dpp v36, v11, v36 row_newbcast:0 row_mask:0xf bank_mask:0xf\n\t"
    "v_mfma_f32_16x16x32_bf16 v[60:63], v[216:219], a[88:91], v[60:63]\n\t"
    "v_exp_f32 v182, v182\n\t"
    "v_mul_f32_dpp v37, v11, v37 row_newbcast:1 row_mask:0xf bank_mask:0xf\n\t"
    "v_mul_f32_dpp v38, v11, v38 row_newbcast:2 row_mask:0xf bank_mask:0xf\n\t"
    "v_mul_f32_dpp v39, v11, v39 row_newbcast:3 row_mask:0xf bank_mask:0xf\n\t"
    "v_mfma_f32_16x16x32_bf16 v[60:63], v[220:223], a[92:95], v[60:63]\n\t"
    "v_exp_f32 v189, v189\n\t"
    "v_cvt_pk_bf16_f32 v39, v38, v39\n\t"
    "v_cvt_pk_bf16_f32 v38, v36, v37\n\t"
    "v_mfma_f32_16x16x32_bf16 v[64:67], v[216:219], a[96:99], v[64:67]\n\t"
    "v_cvt_pk_bf16_f32 v37, v34, v35\n\t"
    "v_cvt_pk_bf16_f32 v36, v32, v33\n\t"
    "ds_write_b128 v224, v[36:39] offset:2048\n\t"
    "v_mfma_f32_16x16x32_bf16 v[64:67], v[220:223], a[100:103], v[64:67]\n\t"
    "ds_read_b64_tr_b16 v[32:33], v225 offset:2048\n\t"
    "ds_read_b64_tr_b16 v[34:35], v226 offset:2048\n\t"
    "s_waitcnt lgkmcnt(3)\n\t"
    "v_mfma_f32_16x16x32_bf16 v[68:71], v[216:219], a[104:107], v[68:71]\n\t"
    "buffer_store_dwordx4 v[28:31], v8, s[52:55], s58 offen offset:64 sc0 sc1\n\t"
    "v_mul_f32_dpp v40, v11, v40 row_newbcast:0 row_mask:0xf bank_mask:0xf\n\t"
    "v_mul_f32_dpp v41, v11, v41 row_newbcast:1 row_mask:0xf bank_mask:0xf\n\t"
    "v_mfma_f32_16x16x32_bf16 v[68:71], v[220:223], a[108:111], v[68:71]\n\t"
    "v_exp_f32 v187, v187\n\t"
    "v_mul_f32_dpp v42, v11, v42 row_newbcast:2 row_mask:0xf bank_mask:0xf\n\t"
    "v_mul_f32_dpp v43, v11, v43 row_newbcast:3 row_mask:0xf bank_mask:0xf\n\t"
    "v_mul_f32_dpp v44, v11, v44 row_newbcast:0 row_mask:0xf bank_mask:0xf\n\t"
    "v_mfma_f32_16x16x32_bf16 v[72:75], v[216:219], a[112:115], v[72:75]\n\t"
    "v_mul_f32_dpp v45, v11, v45 row_newbcast:1 row_mask:0xf bank_mask:0xf\n\t"
    "v_mul_f32_dpp v46, v11, v46 row_newbcast:2 row_mask:0xf bank_mask:0xf\n\t"
    "v_mfma_f32_16x16x32_bf16 v[72:75], v[220:223], a[116:119], v[72:75]\n\t"
    "v_exp_f32 v181, v181\n\t"
    "v_mul_f32_dpp v47, v11, v47 row_newbcast:3 row_mask:0xf bank_mask:0xf\n\t"
    "v_cvt_pk_bf16_f32 v40, v40, v41\n\t"
    "v_cvt_pk_bf16_f32 v41, v42, v43\n\t"
    "v_mfma_f32_16x16x32_bf16 v[76:79], v[216:219], a[120:123], v[76:79]\n\t"
    "v_cvt_pk_bf16_f32 v42, v44, v45\n\t"
    "v_cvt_pk_bf16_f32 v43, v46, v47\n\t"
    "ds_write_b128 v224, v[40:43] offset:3072\n\t"
    "v_mfma_f32_16x16x32_bf16 v[76:79], v[220:223], a[124:127], v[76:79]\n\t"
    "ds_read_b64_tr_b16 v[44:45], v225 offset:3072\n\t"
    "ds_read_b64_tr_b16 v[46:47], v226 offset:3072\n\t"
    "v_smfmac_f32_16x16x64_bf16 v[4:7], a[208:211], v[36:43], v0\n\t"
    "v_smfmac_f32_16x16x64_bf16 a[216:219], a[208:211], v[216:223], v0\n\t"
    "s_waitcnt lgkmcnt(3)\n\t"
    "buffer_store_dwordx4 v[32:35], v8, s[52:55], s58 offen offset:128 sc0 sc1\n\t"
    "v_exp_f32 v186, v186\n\t"
    "v_exp_f32 v180, v180\n\t"
    "v_exp_f32 v184, v184\n\t"
    "v_exp_f32 v178, v178\n\t"
    "v_cvt_pk_bf16_f32 v210, v180, v181\n\t"
    "v_exp_f32 v188, v188\n\t"
    "v_mfma_f32_16x16x32_bf16 v[192:195], a[0:3], a[176:179], v[236:239]\n\t"
    "v_mfma_f32_16x16x32_bf16 v[192:195], a[4:7], a[180:183], v[192:195]\n\t"
    "v_mfma_f32_16x16x32_bf16 v[192:195], a[8:11], a[184:187], v[192:195]\n\t"
    "v_exp_f32 v185, v185\n\t"
    "v_mfma_f32_16x16x32_bf16 v[192:195], a[12:15], a[188:191], v[192:195]\n\t"
    "v_exp_f32 v191, v191\n\t"
    "v_mfma_f32_16x16x32_bf16 v[196:199], a[16:19], a[176:179], v[236:239]\n\t"
    "v_exp_f32 v176, v176\n\t"
    "v_mfma_f32_16x16x32_bf16 v[196:199], a[20:23], a[180:183], v[196:199]\n\t"
    "v_cvt_pk_bf16_f32 v211, v182, v183\n\t"
    "v_cvt_pk_bf16_f32 v214, v188, v189\n\t"
    "v_accvgpr_read_b32 v12, a216\n\t"
    "v_accvgpr_read_b32 v13, a217\n\t"
    "v_add_f32 v12, v12, v13\n\t"
    "v_mfma_f32_16x16x32_bf16 v[196:199], a[24:27], a[184:187], v[196:199]\n\t"
    "v_cmp_nge_f32 vcc, s59, v12\n\t"
    "v_rcp_f32 v12, v12\n\t"
    "v_mfma_f32_16x16x32_bf16 v[196:199], a[28:31], a[188:191], v[196:199]\n\t"
    "v_cvt_pk_bf16_f32 v213, v186, v187\n\t"
    "s_or_b64 s[60:61], s[60:61], vcc\n\t"
    "v_mov_b32 v13, v12\n\t"
    "v_mov_b32_dpp v13, v12 row_shl:4 row_mask:0x2 bank_mask:0xf\n\t"
    "v_mfma_f32_16x16x32_bf16 v[200:203], a[32:35], a[176:179], v[236:239]\n\t"
    "v_mov_b32_dpp v13, v12 row_shl:8 row_mask:0x4 bank_mask:0xf\n\t"
    "v_mov_b32_dpp v13, v12 row_shl:12 row_mask:0x8 bank_mask:0xf\n\t"
    "v_mfma_f32_16x16x32_bf16 v[200:203], a[36:39], a[180:183], v[200:203]\n\t"
    "s_waitcnt lgkmcnt(0)\n\t"
    "buffer_store_dwordx4 v[44:47], v8, s[52:55], s58 offen offset:192 sc0 sc1\n\t"
    "s_add_u32 s58, s58, 4096\n\t"
    "v_mfma_f32_16x16x32_bf16 v[200:203], a[40:43], a[184:187], v[200:203]\n\t"
    "v_exp_f32 v179, v179\n\t"
    "v_mov_b32_dpp v228, v13 row_newbcast:0 row_mask:0xf bank_mask:0xf\n\t"
    "v_mov_b32_dpp v229, v13 row_newbcast:1 row_mask:0xf bank_mask:0xf\n\t"
    "v_mfma_f32_16x16x32_bf16 v[200:203], a[44:47], a[188:191], v[200:203]\n\t"
    "v_exp_f32 v190, v190\n\t"
    "v_mov_b32_dpp v230, v13 row_newbcast:2 row_mask:0xf bank_mask:0xf\n\t"
    "v_mov_b32_dpp v231, v13 row_newbcast:3 row_mask:0xf bank_mask:0xf\n\t"
    "v_pk_mul_f32 v[48:49], v[48:49], v[228:229]\n\t"
    "v_mfma_f32_16x16x32_bf16 v[204:207], a[48:51], a[176:179], v[236:239]\n\t"
    "v_cvt_pk_bf16_f32 v208, v176, v177\n\t"
    "v_cvt_pk_bf16_f32 v209, v178, v179\n\t"
    "v_pk_mul_f32 v[50:51], v[50:51], v[230:231]\n\t"
    "v_pk_mul_f32 v[52:53], v[52:53], v[228:229]\n\t"
    "v_mfma_f32_16x16x32_bf16 v[204:207], a[52:55], a[180:183], v[204:207]\n\t"
    "v_pk_mul_f32 v[54:55], v[54:55], v[230:231]\n\t"
    "v_cvt_pk_bf16_f32 v55, v54, v55\n\t"
    "v_cvt_pk_bf16_f32 v54, v52, v53\n\t"
    "v_mfma_f32_16x16x32_bf16 v[204:207], a[56:59], a[184:187], v[204:207]\n\t"
    "v_cvt_pk_bf16_f32 v212, v184, v185\n\t"
    "v_cvt_pk_bf16_f32 v215, v190, v191\n\t"
    "v_cvt_pk_bf16_f32 v53, v50, v51\n\t"
    "v_cvt_pk_bf16_f32 v52, v48, v49\n\t"
    "v_mfma_f32_16x16x32_bf16 v[204:207], a[60:63], a[188:191], v[204:207]\n\t"
    "ds_write_b128 v224, v[52:55] offset:0\n\t"
    "ds_read_b64_tr_b16 v[48:49], v225 offset:0\n\t"
    "ds_read_b64_tr_b16 v[50:51], v226 offset:0\n\t"
    "v_mfma_f32_16x16x32_bf16 v[80:83], v[208:211], a[252:255], v[80:83]\n\t"
    "v_pk_mul_f32 v[56:57], v[56:57], v[228:229]\n\t"
    "v_pk_mul_f32 v[58:59], v[58:59], v[230:231]\n\t"
    "v_mfma_f32_16x16x32_bf16 v[80:83], v[212:215], a[248:251], v[80:83]\n\t"
    "v_pk_mul_f32 v[60:61], v[60:61], v[228:229]\n\t"
    "v_pk_mul_f32 v[62:63], v[62:63], v[230:231]\n\t"
    "v_cvt_pk_bf16_f32 v56, v56, v57\n\t"
    "v_mfma_f32_16x16x32_bf16 v[84:87], v[208:211], a[244:247], v[84:87]\n\t"
    "v_cvt_pk_bf16_f32 v57, v58, v59\n\t"
    "v_cvt_pk_bf16_f32 v58, v60, v61\n\t"
    "v_mfma_f32_16x16x32_bf16 v[84:87], v[212:215], a[240:243], v[84:87]\n\t"
    "v_cvt_pk_bf16_f32 v59, v62, v63\n\t"
    "ds_write_b128 v224, v[56:59] offset:1024\n\t"
    "v_mfma_f32_16x16x32_bf16 v[88:91], v[208:211], a[236:239], v[88:91]\n\t"
    "ds_read_b64_tr_b16 v[60:61], v225 offset:1024\n\t"
    "ds_read_b64_tr_b16 v[62:63], v226 offset:1024\n\t"
    "v_smfmac_f32_16x16x64_bf16 v[4:7], a[208:211], v[52:59], v0\n\t"
    "v_mfma_f32_16x16x32_bf16 v[88:91], v[212:215], a[232:235], v[88:91]\n\t"
    "s_waitcnt lgkmcnt(3)\n\t"
    "buffer_store_dwordx4 v[48:51], v8, s[52:55], s58 offen sc0 sc1\n\t"
    "v_mfma_f32_16x16x32_bf16 v[92:95], v[208:211], a[88:91], v[92:95]\n\t"
    "v_exp_f32 v196, v196\n\t"
    "v_pk_mul_f32 v[64:65], v[64:65], v[228:229]\n\t"
    "v_pk_mul_f32 v[66:67], v[66:67], v[230:231]\n\t"
    "v_pk_mul_f32 v[68:69], v[68:69], v[228:229]\n\t"
    "v_mfma_f32_16x16x32_bf16 v[92:95], v[212:215], a[92:95], v[92:95]\n\t"
    "v_pk_mul_f32 v[70:71], v[70:71], v[230:231]\n\t"
    "v_cvt_pk_bf16_f32 v71, v70, v71\n\t"
    "v_mfma_f32_16x16x32_bf16 v[96:99], v[208:211], a[96:99], v[96:99]\n\t"
    "v_cvt_pk_bf16_f32 v70, v68, v69\n\t"
    "v_cvt_pk_bf16_f32 v69, v66, v67\n\t"
    "v_cvt_pk_bf16_f32 v68, v64, v65\n\t"
    "v_mfma_f32_16x16x32_bf16 v[96:99], v[212:215], a[100:103], v[96:99]\n\t"
    "v_exp_f32 v205, v205\n\t"
    "ds_write_b128 v224, v[68:71] offset:2048\n\t"
    "ds_read_b64_tr_b16 v[64:65], v225 offset:2048\n\t"
    "v_mfma_f32_16x16x32_bf16 v[100:103], v[208:211], a[104:107], v[100:103]\n\t"
    "v_exp_f32 v194, v194\n\t"
    "ds_read_b64_tr_b16 v[66:67], v226 offset:2048\n\t"
    "s_waitcnt lgkmcnt(3)\n\t"
    "buffer_store_dwordx4 v[60:63], v8, s[52:55], s58 offen offset:64 sc0 sc1\n\t"
    "v_mfma_f32_16x16x32_bf16 v[100:103], v[212:215], a[108:111], v[100:103]\n\t"
    "v_pk_mul_f32 v[72:73], v[72:73], v[228:229]\n\t"
    "v_pk_mul_f32 v[74:75], v[74:75], v[230:231]\n\t"
    "v_mfma_f32_16x16x32_bf16 v[104:107], v[208:211], a[112:115], v[104:107]\n\t"
    "v_pk_mul_f32 v[76:77], v[76:77], v[228:229]\n\t"
    "v_pk_mul_f32 v[78:79], v[78:79], v[230:231]\n\t"
    "v_cvt_pk_bf16_f32 v72, v72, v73\n\t"
    "v_mfma_f32_16x16x32_bf16 v[104:107], v[212:215], a[116:119], v[104:107]\n\t"
    "v_cvt_pk_bf16_f32 v73, v74, v75\n\t"
    "v_cvt_pk_bf16_f32 v74, v76, v77\n\t"
    "v_mfma_f32_16x16x32_bf16 v[108:111], v[208:211], a[120:123], v[108:111]\n\t"
    "v_exp_f32 v199, v199\n\t"
    "v_cvt_pk_bf16_f32 v75, v78, v79\n\t"
    "ds_write_b128 v224, v[72:75] offset:3072\n\t"
    "ds_read_b64_tr_b16 v[76:77], v225 offset:3072\n\t"
    "v_mfma_f32_16x16x32_bf16 v[108:111], v[212:215], a[124:127], v[108:111]\n\t"
    "ds_read_b64_tr_b16 v[78:79], v226 offset:3072\n\t"
    "v_smfmac_f32_16x16x64_bf16 v[4:7], a[208:211], v[68:75], v0\n\t"
    "v_smfmac_f32_16x16x64_bf16 a[220:223], a[208:211], v[208:215], v0\n\t"
    "s_waitcnt lgkmcnt(3)\n\t"
    "buffer_store_dwordx4 v[64:67], v8, s[52:55], s58 offen offset:128 sc0 sc1\n\t"
    "v_exp_f32 v203, v203\n\t"
    "v_exp_f32 v206, v206\n\t"
    "v_exp_f32 v193, v193\n\t"
    "v_exp_f32 v198, v198\n\t"
    "v_exp_f32 v207, v207\n\t"
    "v_exp_f32 v201, v201\n\t"
    "v_cvt_pk_bf16_f32 v223, v206, v207\n\t"
    "v_exp_f32 v197, v197\n\t"
    "v_exp_f32 v192, v192\n\t"
    "s_nop 0\n\t"
    "v_cvt_pk_bf16_f32 v216, v192, v193\n\t"
    "v_cvt_pk_bf16_f32 v218, v196, v197\n\t"
    "v_exp_f32 v204, v204\n\t"
    "v_exp_f32 v200, v200\n\t"
    "v_exp_f32 v202, v202\n\t"
    "v_exp_f32 v195, v195\n\t"
    "s_nop 0\n\t"
    "v_cvt_pk_bf16_f32 v217, v194, v195\n\t"
    "v_cvt_pk_bf16_f32 v222, v204, v205\n\t"
    "v_cvt_pk_bf16_f32 v219, v198, v199\n\t"
    "v_cvt_pk_bf16_f32 v220, v200, v201\n\t"
    "v_cvt_pk_bf16_f32 v221, v202, v203\n\t"
    "v_mfma_f32_16x16x32_bf16 v[124:127], v[216:219], a[88:91], v[124:127]\n\t"
    "v_accvgpr_read_b32 v10, a220\n\t"
    "v_accvgpr_read_b32 v11, a221\n\t"
    "v_add_f32 v10, v10, v11\n\t"
    "v_cmp_nge_f32 vcc, s59, v10\n\t"
    "v_rcp_f32 v10, v10\n\t"
    "v_mfma_f32_16x16x32_bf16 v[124:127], v[220:223], a[92:95], v[124:127]\n\t"
    "s_or_b64 s[60:61], s[60:61], vcc\n\t"
    "v_mov_b32 v11, v10\n\t"
    "v_mov_b32_dpp v11, v10 row_shl:4 row_mask:0x2 bank_mask:0xf\n\t"
    "v_mov_b32_dpp v11, v10 row_shl:8 row_mask:0x4 bank_mask:0xf\n\t"
    "v_mfma_f32_16x16x32_bf16 v[128:131], v[216:219], a[96:99], v[128:131]\n\t"
    "v_mov_b32_dpp v11, v10 row_shl:12 row_mask:0x8 bank_mask:0xf\n\t"
    "s_waitcnt lgkmcnt(0)\n\t"
    "buffer_store_dwordx4 v[76:79], v8, s[52:55], s58 offen offset:192 sc0 sc1\n\t"
    "s_add_u32 s58, s58, 4096\n\t"
    "v_mfma_f32_16x16x32_bf16 v[128:131], v[220:223], a[100:103], v[128:131]\n\t"
    "v_mov_b32_dpp v232, v11 row_newbcast:0 row_mask:0xf bank_mask:0xf\n\t"
    "v_mov_b32_dpp v233, v11 row_newbcast:1 row_mask:0xf bank_mask:0xf\n\t"
    "v_mov_b32_dpp v234, v11 row_newbcast:2 row_mask:0xf bank_mask:0xf\n\t"
    "v_mov_b32_dpp v235, v11 row_newbcast:3 row_mask:0xf bank_mask:0xf\n\t"
    "v_mfma_f32_16x16x32_bf16 v[132:135], v[216:219], a[104:107], v[132:135]\n\t"
    "v_pk_mul_f32 v[80:81], v[80:81], v[232:233]\n\t"
    "v_pk_mul_f32 v[82:83], v[82:83], v[234:235]\n\t"
    "v_pk_mul_f32 v[84:85], v[84:85], v[232:233]\n\t"
    "v_pk_mul_f32 v[86:87], v[86:87], v[234:235]\n\t"
    "v_mfma_f32_16x16x32_bf16 v[132:135], v[220:223], a[108:111], v[132:135]\n\t"
    "v_cvt_pk_bf16_f32 v87, v86, v87\n\t"
    "v_cvt_pk_bf16_f32 v86, v84, v85\n\t"
    "v_cvt_pk_bf16_f32 v85, v82, v83\n\t"
    "v_cvt_pk_bf16_f32 v84, v80, v81\n\t"
    "v_mfma_f32_16x16x32_bf16 v[136:139], v[216:219], a[112:115], v[136:139]\n\t"
    "ds_write_b128 v224, v[84:87] offset:0\n\t"
    "ds_read_b64_tr_b16 v[80:81], v225 offset:0\n\t"
    "ds_read_b64_tr_b16 v[82:83], v226 offset:0\n\t"
    "v_pk_mul_f32 v[88:89], v[88:89], v[232:233]\n\t"
    "v_mfma_f32_16x16x32_bf16 v[136:139], v[220:223], a[116:119], v[136:139]\n\t"
    "v_pk_mul_f32 v[90:91], v[90:91], v[234:235]\n\t"
    "v_pk_mul_f32 v[92:93], v[92:93], v[232:233]\n\t"
    "v_pk_mul_f32 v[94:95], v[94:95], v[234:235]\n\t"
    "v_cvt_pk_bf16_f32 v88, v88, v89\n\t"
    "v_mfma_f32_16x16x32_bf16 v[140:143], v[216:219], a[120:123], v[140:143]\n\t"
    "v_cvt_pk_bf16_f32 v89, v90, v91\n\t"
    "v_cvt_pk_bf16_f32 v90, v92, v93\n\t"
    "v_cvt_pk_bf16_f32 v91, v94, v95\n\t"
    "ds_write_b128 v224, v[88:91] offset:1024\n\t"
    "v_mfma_f32_16x16x32_bf16 v[140:143], v[220:223], a[124:127], v[140:143]\n\t"
    "ds_read_b64_tr_b16 v[92:93], v225 offset:1024\n\t"
    "ds_read_b64_tr_b16 v[94:95], v226 offset:1024\n\t"
    "v_smfmac_f32_16x16x64_bf16 v[4:7], a[208:211], v[84:91], v0\n\t"
    "s_waitcnt lgkmcnt(3)\n\t"
    "v_mfma_f32_16x16x32_bf16 v[112:115], v[216:219], a[252:255], v[112:115]\n\t"
    "buffer_store_dwordx4 v[80:83], v8, s[52:55], s58 offen sc0 sc1\n\t"
    "v_pk_mul_f32 v[96:97], v[96:97], v[232:233]\n\t"
    "v_pk_mul_f32 v[98:99], v[98:99], v[234:235]\n\t"
    "v_pk_mul_f32 v[100:101], v[100:101], v[232:233]\n\t"
    "v_mfma_f32_16x16x32_bf16 v[112:115], v[220:223], a[248:251], v[112:115]\n\t"
    "v_pk_mul_f32 v[102:103], v[102:103], v[234:235]\n\t"
    "v_cvt_pk_bf16_f32 v103, v102, v103\n\t"
    "v_cvt_pk_bf16_f32 v102, v100, v101\n\t"
    "v_cvt_pk_bf16_f32 v101, v98, v99\n\t"
    "v_mfma_f32_16x16x32_bf16 v[116:119], v[216:219], a[244:247], v[116:119]\n\t"
    "v_cvt_pk_bf16_f32 v100, v96, v97\n\t"
    "ds_write_b128 v224, v[100:103] offset:2048\n\t"
    "ds_read_b64_tr_b16 v[96:97], v225 offset:2048\n\t"
    "ds_read_b64_tr_b16 v[98:99], v226 offset:2048\n\t"
    "v_mfma_f32_16x16x32_bf16 v[116:119], v[220:223], a[240:243], v[116:119]\n\t"
    "s_waitcnt lgkmcnt(3)\n\t"
    "buffer_store_dwordx4 v[92:95], v8, s[52:55], s58 offen offset:64 sc0 sc1\n\t"
    "v_pk_mul_f32 v[104:105], v[104:105], v[232:233]\n\t"
    "v_pk_mul_f32 v[106:107], v[106:107], v[234:235]\n\t"
    "v_mfma_f32_16x16x32_bf16 v[120:123], v[216:219], a[236:239], v[120:123]\n\t"
    "v_pk_mul_f32 v[108:109], v[108:109], v[232:233]\n\t"
    "v_pk_mul_f32 v[110:111], v[110:111], v[234:235]\n\t"
    "v_cvt_pk_bf16_f32 v104, v104, v105\n\t"
    "v_cvt_pk_bf16_f32 v105, v106, v107\n\t"
    "v_mfma_f32_16x16x32_bf16 v[120:123], v[220:223], a[232:235], v[120:123]\n\t"
    "v_cvt_pk_bf16_f32 v106, v108, v109\n\t"
    "v_cvt_pk_bf16_f32 v107, v110, v111\n\t"
    "ds_write_b128 v224, v[104:107] offset:3072\n\t"
    "ds_read_b64_tr_b16 v[108:109], v225 offset:3072\n\t"
    "v_smfmac_f32_16x16x64_bf16 a[224:227], a[208:211], v[216:223], v0\n\t"
    "ds_read_b64_tr_b16 v[110:111], v226 offset:3072\n\t"
    "v_smfmac_f32_16x16x64_bf16 v[4:7], a[208:211], v[100:107], v0\n\t"
    "s_waitcnt lgkmcnt(3)\n\t"
    "buffer_store_dwordx4 v[96:99], v8, s[52:55], s58 offen offset:128 sc0 sc1\n\t"
    "s_nop 3\n\t"
    "v_accvgpr_read_b32 v12, a224\n\t"
    "v_accvgpr_read_b32 v13, a225\n\t"
    "v_add_f32 v12, v12, v13\n\t"
    "v_cmp_nge_f32 vcc, s59, v12\n\t"
    "v_rcp_f32 v12, v12\n\t"
    "s_or_b64 s[60:61], s[60:61], vcc\n\t"
    "v_mov_b32 v13, v12\n\t"
    "v_mov_b32_dpp v13, v12 row_shl:4 row_mask:0x2 bank_mask:0xf\n\t"
    "v_mov_b32_dpp v13, v12 row_shl:8 row_mask:0x4 bank_mask:0xf\n\t"
    "v_mov_b32_dpp v13, v12 row_shl:12 row_mask:0x8 bank_mask:0xf\n\t"
    "s_waitcnt lgkmcnt(0)\n\t"
    "buffer_store_dwordx4 v[108:111], v8, s[52:55], s58 offen offset:192 sc0 sc1\n\t"
    "s_add_u32 s58, s58, 4096\n\t"
    "v_mov_b32_dpp v236, v13 row_newbcast:0 row_mask:0xf bank_mask:0xf\n\t"
    "v_mov_b32_dpp v237, v13 row_newbcast:1 row_mask:0xf bank_mask:0xf\n\t"
    "v_mov_b32_dpp v238, v13 row_newbcast:2 row_mask:0xf bank_mask:0xf\n\t"
    "v_mov_b32_dpp v239, v13 row_newbcast:3 row_mask:0xf bank_mask:0xf\n\t"
    "v_pk_mul_f32 v[112:113], v[112:113], v[236:237]\n\t"
    "v_pk_mul_f32 v[114:115], v[114:115], v[238:239]\n\t"
    "v_pk_mul_f32 v[116:117], v[116:117], v[236:237]\n\t"
    "v_pk_mul_f32 v[118:119], v[118:119], v[238:239]\n\t"
    "v_cvt_pk_bf16_f32 v119, v118, v119\n\t"
    "v_cvt_pk_bf16_f32 v118, v116, v117\n\t"
    "v_cvt_pk_bf16_f32 v117, v114, v115\n\t"
    "v_cvt_pk_bf16_f32 v116, v112, v113\n\t"
    "ds_write_b128 v224, v[116:119] offset:0\n\t"
    "ds_read_b64_tr_b16 v[112:113], v225 offset:0\n\t"
    "ds_read_b64_tr_b16 v[114:115], v226 offset:0\n\t"
    "v_pk_mul_f32 v[120:121], v[120:121], v[236:237]\n\t"
    "v_pk_mul_f32 v[122:123], v[122:123], v[238:239]\n\t"
    "v_pk_mul_f32 v[124:125], v[124:125], v[236:237]\n\t"
    "v_pk_mul_f32 v[126:127], v[126:127], v[238:239]\n\t"
    "v_cvt_pk_bf16_f32 v120, v120, v121\n\t"
    "v_cvt_pk_bf16_f32 v121, v122, v123\n\t"
    "v_cvt_pk_bf16_f32 v122, v124, v125\n\t"
    "v_cvt_pk_bf16_f32 v123, v126, v127\n\t"
    "ds_write_b128 v224, v[120:123] offset:1024\n\t"
    "ds_read_b64_tr_b16 v[124:125], v225 offset:1024\n\t"
    "ds_read_b64_tr_b16 v[126:127], v226 offset:1024\n\t"
    "v_smfmac_f32_16x16x64_bf16 v[4:7], a[208:211], v[116:123], v0\n\t"
    "s_waitcnt lgkmcnt(3)\n\t"
    "buffer_store_dwordx4 v[112:115], v8, s[52:55], s58 offen sc0 sc1\n\t"
    "v_pk_mul_f32 v[128:129], v[128:129], v[236:237]\n\t"
    "v_pk_mul_f32 v[130:131], v[130:131], v[238:239]\n\t"
    "v_pk_mul_f32 v[132:133], v[132:133], v[236:237]\n\t"
    "v_pk_mul_f32 v[134:135], v[134:135], v[238:239]\n\t"
    "v_cvt_pk_bf16_f32 v135, v134, v135\n\t"
    "v_cvt_pk_bf16_f32 v134, v132, v133\n\t"
    "v_cvt_pk_bf16_f32 v133, v130, v131\n\t"
    "v_cvt_pk_bf16_f32 v132, v128, v129\n\t"
    "ds_write_b128 v224, v[132:135] offset:2048\n\t"
    "ds_read_b64_tr_b16 v[128:129], v225 offset:2048\n\t"
    "ds_read_b64_tr_b16 v[130:131], v226 offset:2048\n\t"
    "s_waitcnt lgkmcnt(3)\n\t"
    "buffer_store_dwordx4 v[124:127], v8, s[52:55], s58 offen offset:64 sc0 sc1\n\t"
    "v_pk_mul_f32 v[136:137], v[136:137], v[236:237]\n\t"
    "v_pk_mul_f32 v[138:139], v[138:139], v[238:239]\n\t"
    "v_pk_mul_f32 v[140:141], v[140:141], v[236:237]\n\t"
    "v_pk_mul_f32 v[142:143], v[142:143], v[238:239]\n\t"
    "v_cvt_pk_bf16_f32 v136, v136, v137\n\t"
    "v_cvt_pk_bf16_f32 v137, v138, v139\n\t"
    "v_cvt_pk_bf16_f32 v138, v140, v141\n\t"
    "v_cvt_pk_bf16_f32 v139, v142, v143\n\t"
    "ds_write_b128 v224, v[136:139] offset:3072\n\t"
    "ds_read_b64_tr_b16 v[140:141], v225 offset:3072\n\t"
    "ds_read_b64_tr_b16 v[142:143], v226 offset:3072\n\t"
    "v_smfmac_f32_16x16x64_bf16 v[4:7], a[208:211], v[132:139], v0\n\t"
    "s_waitcnt lgkmcnt(3)\n\t"
    "buffer_store_dwordx4 v[128:131], v8, s[52:55], s58 offen offset:128 sc0 sc1\n\t"
    "s_waitcnt lgkmcnt(0)\n\t"
    "buffer_store_dwordx4 v[140:143], v8, s[52:55], s58 offen offset:192 sc0 sc1\n\t"
    "s_nop 3\n\t"
    "v_add_f32 v4, v4, v5\n\t"
    "v_mov_b32 v9, 0x207\n\t"
    "v_cmp_class_f32 vcc, v4, v9\n\t"
    "s_nop 1\n\t"
    "s_or_b64 s[60:61], s[60:61], vcc\n\t"
    "s_cmp_lg_u64 s[60:61], 0\n\t"
    "s_cbranch_scc1 DONE_%=\n\t"
    "s_endpgm\n\t"
    "DONE_%=:\n\t"
    "s_cmp_lg_u64 s[60:61], 0\n\t"
    "s_cselect_b32 %[flag], 1, 0\n\t"
    : [flag] "=s"(flag)
    : [q] "s"(qp), [k] "s"(kp), [v] "s"(vp), [o] "s"(op), [lds] "s"(ldsb), [wid] "s"(wid)
    : "v0", "v1", "v2", "v3", "v4", "v5", "v6", "v7", "v8", "v9", "v10", "v11", "v12", "v13", "v14", "v15", "v16", "v17", "v18", "v19", "v20", "v21", "v22", "v23", "v24", "v25", "v26", "v27", "v28", "v29", "v30", "v31", "v32", "v33", "v34", "v35", "v36", "v37", "v38", "v39", "v40", "v41", "v42", "v43", "v44", "v45", "v46", "v47", "v48", "v49", "v50", "v51", "v52", "v53", "v54", "v55", "v56", "v57", "v58", "v59", "v60", "v61", "v62", "v63", "v64", "v65", "v66", "v67", "v68", "v69", "v70", "v71", "v72", "v73", "v74", "v75", "v76", "v77", "v78", "v79", "v80", "v81", "v82", "v83", "v84", "v85", "v86", "v87", "v88", "v89", "v90", "v91", "v92", "v93", "v94", "v95", "v96", "v97", "v98", "v99", "v100", "v101", "v102", "v103", "v104", "v105", "v106", "v107", "v108", "v109", "v110", "v111", "v112", "v113", "v114", "v115", "v116", "v117", "v118", "v119", "v120", "v121", "v122", "v123", "v124", "v125", "v126", "v127", "v128", "v129", "v130", "v131", "v132", "v133", "v134", "v135", "v136", "v137", "v138", "v139", "v140", "v141", "v142", "v143", "v144", "v145", "v146", "v147", "v148", "v149", "v150", "v151", "v152", "v153", "v154", "v155", "v156", "v157", "v158", "v159", "v160", "v161", "v162", "v163", "v164", "v165", "v166", "v167", "v168", "v169", "v170", "v171", "v172", "v173", "v174", "v175", "v176", "v177", "v178", "v179", "v180", "v181", "v182", "v183", "v184", "v185", "v186", "v187", "v188", "v189", "v190", "v191", "v192", "v193", "v194", "v195", "v196", "v197", "v198", "v199", "v200", "v201", "v202", "v203", "v204", "v205", "v206", "v207", "v208", "v209", "v210", "v211", "v212", "v213", "v214", "v215", "v216", "v217", "v218", "v219", "v220", "v221", "v222", "v223", "v224", "v225", "v226", "v227", "v228", "v229", "v230", "v231", "v232", "v233", "v234", "v235", "v236", "v237", "v238", "v239", "v240", "v241", "v242", "v243", "v244", "v245", "v246", "v247", "v248", "v249", "v250", "v251", "v252", "v253", "v254", "v255", "a0", "a1", "a2", "a3", "a4", "a5", "a6", "a7", "a8", "a9", "a10", "a11", "a12", "a13", "a14", "a15", "a16", "a17", "a18", "a19", "a20", "a21", "a22", "a23", "a24", "a25", "a26", "a27", "a28", "a29", "a30", "a31", "a32", "a33", "a34", "a35", "a36", "a37", "a38", "a39", "a40", "a41", "a42", "a43", "a44", "a45", "a46", "a47", "a48", "a49", "a50", "a51", "a52", "a53", "a54", "a55", "a56", "a57", "a58", "a59", "a60", "a61", "a62", "a63", "a64", "a65", "a66", "a67", "a68", "a69", "a70", "a71", "a72", "a73", "a74", "a75", "a76", "a77", "a78", "a79", "a80", "a81", "a82", "a83", "a84", "a85", "a86", "a87", "a88", "a89", "a90", "a91", "a92", "a93", "a94", "a95", "a96", "a97", "a98", "a99", "a100", "a101", "a102", "a103", "a104", "a105", "a106", "a107", "a108", "a109", "a110", "a111", "a112", "a113", "a114", "a115", "a116", "a117", "a118", "a119", "a120", "a121", "a122", "a123", "a124", "a125", "a126", "a127", "a128", "a129", "a130", "a131", "a132", "a133", "a134", "a135", "a136", "a137", "a138", "a139", "a140", "a141", "a142", "a143", "a144", "a145", "a146", "a147", "a148", "a149", "a150", "a151", "a152", "a153", "a154", "a155", "a156", "a157", "a158", "a159", "a160", "a161", "a162", "a163", "a164", "a165", "a166", "a167", "a168", "a169", "a170", "a171", "a172", "a173", "a174", "a175", "a176", "a177", "a178", "a179", "a180", "a181", "a182", "a183", "a184", "a185", "a186", "a187", "a188", "a189", "a190", "a191", "a192", "a193", "a194", "a195", "a196", "a197", "a198", "a199", "a200", "a201", "a202", "a203", "a204", "a205", "a206", "a207", "a208", "a209", "a210", "a211", "a212", "a213", "a214", "a215", "a216", "a217", "a218", "a219", "a220", "a221", "a222", "a223", "a224", "a225", "a226", "a227", "a228", "a229", "a230", "a231", "a232", "a233", "a234", "a235", "a236", "a237", "a238", "a239", "a240", "a241", "a242", "a243", "a244", "a245", "a246", "a247", "a248", "a249", "a250", "a251", "a252", "a253", "a254", "a255", "s40", "s41", "s42", "s43", "s44", "s45", "s46", "s47", "s48", "s49", "s50", "s51", "s52", "s53", "s54", "s55", "s56", "s57", "s58", "s59", "s60", "s61", "s62", "s63", "s64", "s65", "s66", "s67", "vcc", "memory", "scc");
  if (flag) {
    fallback_rows((const unsigned short*)qp, (const unsigned short*)kp, (const unsigned short*)vp, (unsigned short*)op,
                  (unsigned short*)(lds + 131072 + 8192 * wid), 80 * (int)wid, wid < 3 ? 80 : 64);
  }
}

extern "C" void kernel_launch(void* const* d_in, const int* in_sizes, int n_in, void* d_out, int out_size, void* d_ws, size_t ws_size, hipStream_t stream) {
  attn_fwd<<<256, 256, 0, stream>>>((const char*)d_in[0], (const char*)d_in[1], (const char*)d_in[2], (char*)d_out );
}
```
